```python
import torch

def _softmax(x, axis=-1):
    return torch.softmax(x, dim=axis)

def _gelu(x):
    return torch.nn.functional.gelu(x, approximate='tanh')

def _silu(x):
    return torch.nn.functional.silu(x)

def _sigmoid(x):
    return torch.sigmoid(x)

def _layernorm(x, axis=-1, eps=1e-05):
    m = x.mean(dim=axis, keepdim=True)
    v = x.var(dim=axis, keepdim=True, unbiased=False)
    return (x - m) / torch.sqrt(v + eps)

def _scatter_add(tgt, idx, src, axis=0):
    return tgt.clone().index_add_(axis, idx, src)

def _segment_sum(src, seg, n):
    out = torch.zeros((n,) + src.shape[1:], dtype=src.dtype)
    return out.index_add_(0, seg, src)

def _pad(x, pw):
    flat = tuple((v for pair in reversed(pw) for v in pair))
    return torch.nn.functional.pad(x, flat)
kernel_name = 'adaLN_cond__gqa_block_cached__glu_gated_geglu__decode'

def setup_inputs():

    _w = lambda a: (lambda t: t.to(torch.bfloat16) if t.is_floating_point() else t)(torch.as_tensor(a))

    def _A_setup():
        B, Sq, D = (1, 1, 640)
        Hq, Hkv, Sk, Dh = (32, 32, 4096, 128)
        assert Hq % Hkv == 0, (Hq, Hkv)
        return {'x': _w((lambda *s: torch.randn(*s, dtype=torch.bfloat16) * 0.05)(B, Sq, D)), 'k_cache': _w((lambda *s: torch.randn(*s, dtype=torch.bfloat16) * 3.2)(B, Hkv, Sk, Dh)), 'v_cache': _w((lambda *s: torch.randn(*s, dtype=torch.bfloat16) * 3.2)(B, Hkv, Sk, Dh)), 'wq': _w((lambda *s: torch.randn(*s, dtype=torch.bfloat16) * 3.2)(D, Hq * Dh) * D ** (-0.5)), 'wo': _w((lambda *s: torch.randn(*s, dtype=torch.bfloat16) * 3.2)(Hq * Dh, D) * (Hq * Dh) ** (-0.5))}
    _A_in = _A_setup()

    def _B_setup():
        B, S, Dm, Df = (1, 1, 640, 1728)
        return {'x': _w((lambda *s: torch.randn(*s, dtype=torch.bfloat16) * 0.05)(B, S, Dm)), 'w_gate': _w((lambda *s: torch.randn(*s, dtype=torch.bfloat16) * 3.2)(Dm, Df) * Dm ** (-0.5)), 'w_up': _w((lambda *s: torch.randn(*s, dtype=torch.bfloat16) * 3.2)(Dm, Df) * Dm ** (-0.5)), 'w_down': _w((lambda *s: torch.randn(*s, dtype=torch.bfloat16) * 3.2)(Df, Dm) * Df ** (-0.5))}
    _B_in = _B_setup()
    return {'A_x': _A_in['x'], 'A_k_cache': _A_in['k_cache'], 'A_v_cache': _A_in['v_cache'], 'A_wq': _A_in['wq'], 'A_wo': _A_in['wo'], 'B_x': _B_in['x'], 'B_w_gate': _B_in['w_gate'], 'B_w_up': _B_in['w_up'], 'B_w_down': _B_in['w_down'], 'c': _w((lambda *s: torch.randn(*s, dtype=torch.bfloat16) * 0.1)(1, 640) * 8.0), 'w_mod': _w((lambda *s: torch.randn(*s, dtype=torch.bfloat16) * 0.1)(640, 6 * 640) * 640 ** (-0.5) * 8.0)}

def reference(A_x, A_k_cache, A_v_cache, A_wq, A_wo, B_x, B_w_gate, B_w_up, B_w_down, c, w_mod):

    def _A_ref(x, k_cache, v_cache, wq, wo):
        B, Sq, D = x.shape
        Hq, Dh = (32, 128)
        Hkv = k_cache.shape[1]
        rep = Hq // Hkv
        q = (lambda x, *d: x.permute(*d))((x @ wq).reshape(B, Sq, Hq, Dh), 0, 2, 1, 3)
        k = (lambda x, n, axis: torch.repeat_interleave(x, n, dim=axis))(k_cache, rep, axis=1)
        v = (lambda x, n, axis: torch.repeat_interleave(x, n, dim=axis))(v_cache, rep, axis=1)
        scores = torch.einsum('bhqd,bhkd->bhqk', q, k) * Dh ** (-0.5)
        p = _softmax(scores, axis=-1)
        o = torch.einsum('bhqk,bhkd->bhqd', p, v)
        return (lambda x, *d: x.permute(*d))(o, 0, 2, 1, 3).reshape(B, Sq, Hq * Dh) @ wo

    def _B_ref(x, w_gate, w_up, w_down):
        g = torch.einsum('bsd,df->bsf', x, w_gate)
        u = torch.einsum('bsd,df->bsf', x, w_up)
        h = _gelu(g) * u
        return torch.einsum('bsf,fd->bsd', h, w_down)
    x = A_x
    mod = (_silu(c) @ w_mod).reshape(c.shape[0], 6, 1, -1)
    sa, ca, ga, sb, cb, gb = (mod[:, 0].clone(), mod[:, 1].clone(), mod[:, 2].clone(), mod[:, 3].clone(), mod[:, 4].clone(), mod[:, 5].clone())
    h = _layernorm(x, axis=-1) * (1 + ca) + sa
    x = x + ga * _A_ref(h, A_k_cache, A_v_cache, A_wq, A_wo)
    h = _layernorm(x, axis=-1) * (1 + cb) + sb
    x = x + gb * _B_ref(h, B_w_gate, B_w_up, B_w_down)
    return x
```

```cpp
#include <hip/hip_runtime.h>
#include <hip/hip_bf16.h>

typedef unsigned int u32;
typedef unsigned long long u64;
typedef unsigned short u16;
typedef __attribute__((ext_vector_type(4))) u32 u32x4;
typedef __attribute__((ext_vector_type(2))) u32 u32x2;
typedef __attribute__((ext_vector_type(4))) float f32x4;
typedef __attribute__((ext_vector_type(8))) __bf16 bf16x8;
typedef __attribute__((ext_vector_type(2))) __bf16 bf16x2;
typedef __attribute__((ext_vector_type(2))) float f32x2;
#define DEV __device__ __forceinline__

#define WIN 5
#define WINC 4
#define NAP1 8
#define DRY 1
#define NT_KV 1
#define NT_W 1

constexpr int D = 640, DH = 128, SK = 4096, DF = 1728, NMOD = 3840;
constexpr int N_SACA = 80, B_MOD = 80, B_WO = 120, B_MLP = 200, N_MLP = 54, N_FIN = 40;
constexpr u32 WS_EPOCH = 0, WS_FLAG = 4096, WS_H1 = 8192, WS_MOD = 16384, WS_QP = 65536, WS_OP = 196608,
              WS_ML = 327680, WS_WOP = 344064, WS_DP = 393216, WS_BYTES = 655360, WS_STAMP = 1 << 20;
constexpr u32 EMPTY = 0xFFFFFFFFu;
constexpr u32 SPIN_CAP = 1u << 22;
constexpr int SC1 = 16;

struct Params {
  const u16 *x, *kc, *vc, *wq, *wo, *wg, *wu, *wd, *c, *wm;
  u16* out;
  char* ws;
};

DEV float bf2f(u32 v) { return __uint_as_float(v << 16); }
DEV u32 pk2(float lo, float hi) { f32x2 f = {lo, hi}; return __builtin_bit_cast(u32, __builtin_convertvector(f, bf16x2)); }
DEV u32 f2bf(float f) { return pk2(f, 0.f) & 0xFFFFu; }
DEV float rbf(float f) { return __uint_as_float(pk2(0.f, f) & 0xFFFF0000u); }
DEV u32 canon(float f) { u32 u = __float_as_uint(f); return u == EMPTY ? 0x7FFFFFFFu : u; }
template <int C> DEV float dpp(float v) { return __int_as_float(__builtin_amdgcn_update_dpp(0, __float_as_int(v), C, 0xF, 0xF, true)); }
template <int C> DEV u32 dppu(u32 v) { return (u32)__builtin_amdgcn_update_dpp(0, (int)v, C, 0xF, 0xF, true); }
DEV float sum16(float v) { u32 u = __float_as_uint(v); auto r = __builtin_amdgcn_permlane16_swap(u, u, false, false); return __uint_as_float(r[0]) + __uint_as_float(r[1]); }
DEV float sum32(float v) { u32 u = __float_as_uint(v); auto r = __builtin_amdgcn_permlane32_swap(u, u, false, false); return __uint_as_float(r[0]) + __uint_as_float(r[1]); }
DEV float max16(float v) { u32 u = __float_as_uint(v); auto r = __builtin_amdgcn_permlane16_swap(u, u, false, false); return fmaxf(__uint_as_float(r[0]), __uint_as_float(r[1])); }
DEV float max32(float v) { u32 u = __float_as_uint(v); auto r = __builtin_amdgcn_permlane32_swap(u, u, false, false); return fmaxf(__uint_as_float(r[0]), __uint_as_float(r[1])); }
DEV float quad_sum(float v) { v += dpp<0xB1>(v); v += dpp<0x4E>(v); return v; }
DEV float top_sum(float v) { v += dpp<0x124>(v); v += dpp<0x128>(v); return sum32(sum16(v)); }
DEV float top_max(float v) { v = fmaxf(v, dpp<0x124>(v)); v = fmaxf(v, dpp<0x128>(v)); return max32(max16(v)); }
DEV float wave_sum(float v) { return top_sum(quad_sum(v)); }
DEV f32x4 mfma(u32x4 a, u32x4 b, f32x4 c) { return __builtin_amdgcn_mfma_f32_16x16x32_bf16(__builtin_bit_cast(bf16x8, a), __builtin_bit_cast(bf16x8, b), c, 0, 0, 0); }
DEV u32x4 mkA(u32 w, u32x4 s) { u32x4 a; a.x = __builtin_amdgcn_perm(w, w, s.x); a.y = __builtin_amdgcn_perm(w, w, s.y); a.z = __builtin_amdgcn_perm(w, w, s.z); a.w = __builtin_amdgcn_perm(w, w, s.w); return a; }
DEV u32x4 mksel(int j, int hi) { u32 nz = (j & 1) ? (hi ? 0x03020c0cu : 0x01000c0cu) : (hi ? 0x0c0c0302u : 0x0c0c0100u); int q = j >> 1; u32 z = 0x0c0c0c0cu;
  u32x4 s = {q == 0 ? nz : z, q == 1 ? nz : z, q == 2 ? nz : z, q == 3 ? nz : z}; return s; }
template <int NT> DEV u32x4 ldw(const u16* p) { if (NT) return __builtin_nontemporal_load((const u32x4*)p); else return *(const u32x4*)p; }
DEV void fence() { asm volatile("" ::: "memory"); }
DEV void wg_barrier() { asm volatile("s_waitcnt lgkmcnt(0)\n\ts_barrier" ::: "memory"); }
DEV void lds_wait() { asm volatile("s_waitcnt lgkmcnt(0)" ::: "memory"); }
typedef __amdgpu_buffer_rsrc_t rsrc_t;
DEV u32x4 ld4(rsrc_t r, u32 off) { return __builtin_amdgcn_raw_buffer_load_b128(r, off, 0, SC1); }
DEV u32x2 ld2(rsrc_t r, u32 off) { return __builtin_amdgcn_raw_buffer_load_b64(r, off, 0, SC1); }
DEV void st4(rsrc_t r, u32 off, u32x4 v) { __builtin_amdgcn_raw_buffer_store_b128(v, r, off, 0, SC1); }
DEV void st2(rsrc_t r, u32 off, u32x2 v) { __builtin_amdgcn_raw_buffer_store_b64(v, r, off, 0, SC1); }
DEV void st1(rsrc_t r, u32 off, u32 v) { __builtin_amdgcn_raw_buffer_store_b32(v, r, off, 0, SC1); }
DEV bool full4(u32x4 v) { return v.x != EMPTY && v.y != EMPTY && v.z != EMPTY && v.w != EMPTY; }
DEV f32x4 asf(u32x4 v) { return __builtin_bit_cast(f32x4, v); }
DEV u32x4 canon4(f32x4 v) { u32x4 r = {canon(v.x), canon(v.y), canon(v.z), canon(v.w)}; return r; }
#define SPIN(nap, cap, cond) _Pragma("unroll 1") for (u32 spin_ = 0; spin_ < (cap); ++spin_) { fence(); cond; if (__all(ok)) break; __builtin_amdgcn_s_sleep(nap); }
#define TAGOK(v) ((((v).x ^ tagLo) >> 16) == 0 && (v).y == tagHi)
#define UNR _Pragma("unroll")
#define FORGET_R UNR for (int i_ = 0; i_ < 32; ++i_) asm volatile("" : "=v"(R[i_]))
#define LANE_VARS int lane = tid & 63; asm volatile("" : "+v"(lane)); const int g = lane >> 4, n = lane & 15, t = n >> 3, r20 = 160 * cw + 20 * (2 * g + t); \
  const u32x4 selL = mksel(n & 7, 0), selH = mksel(n & 7, 1); (void)r20; (void)selL; (void)selH
#define THROTTLE(w) asm volatile("s_waitcnt vmcnt(%0)" ::"n"(w) : "memory")
DEV int pairmap(int i, int n) { return i < (n & ~15) ? ((i & ~15) | ((i & 7) << 1) | ((i >> 3) & 1)) : i; }
DEV float silu_bf(float v) { return v * __builtin_amdgcn_rcpf(1.f + __expf(-v)); }
#define ST(k)

struct Lds {
  float red[8][128];
  float ored[8][2][128];
  float ml[8][2];
  float x2[D];
  float modv[3][D];
  u32 q[64];
  u32 vec[12][96];
  u32 cnt[4];
};

DEV void cbar(Lds& L, u32& target, int lane) {
  target += 4;
  lds_wait();
  if (lane == 0) __hip_atomic_fetch_add(&L.cnt[0], 1u, __ATOMIC_RELAXED, __HIP_MEMORY_SCOPE_WORKGROUP);
#pragma unroll 1
  for (u32 s = 0; s < SPIN_CAP; ++s) { fence(); if (__hip_atomic_load(&L.cnt[0], __ATOMIC_RELAXED, __HIP_MEMORY_SCOPE_WORKGROUP) >= target) break; }
  fence();
}
DEV void ln_stats(const float (&v)[10], float& mean_b, float& sd_b) {
  float s = 0.f;
#pragma unroll
  for (int i = 0; i < 10; ++i) s += v[i];
  const float m = wave_sum(s) * (1.f / D);
  float q = 0.f;
#pragma unroll
  for (int i = 0; i < 10; ++i) { const float d = v[i] - m; q += d * d; }
  const float var = rbf(wave_sum(q) * (1.f / D));
  mean_b = rbf(m);
  sd_b = rbf(sqrtf(rbf(var + 1e-5f)));
}
DEV float ln_mod(float x, float mean_b, float sd_b, float sc, float sh) { return rbf(rbf(rbf(x - mean_b) / sd_b) * rbf(1.f + sc)) + sh; }

__global__ __launch_bounds__(768, 3) void adaln_block_kernel(Params P) {
  __shared__ Lds L;
  const int tid = threadIdx.x, b = blockIdx.x;
  const int wave = __builtin_amdgcn_readfirstlane(tid >> 6);
  const int h = b >> 3, s = b & 7;
  const bool ctrl = wave < 4, isSaca = b < N_SACA, isMod = b >= B_MOD && b < B_WO, isWo = b >= B_WO && b < B_MLP, isMlp = b >= B_MLP && b < B_MLP + N_MLP;
  const int cw = wave & 3, sw = wave - 4;
  const rsrc_t ws = __builtin_amdgcn_make_buffer_rsrc(P.ws, 0, WS_BYTES, 0x00020000);
  const f32x4 zero4 = {0.f, 0.f, 0.f, 0.f};
  const int wp = (b - B_WO) / 10, wc = (b - B_WO) % 10, fi = b - B_MLP, f0 = 32 * pairmap(fi, N_MLP);
  u32x4 R[32];
  u32 nb = 0;
  ST(0);
  const u32x2 ep = ld2(ws, WS_EPOCH);
  u64 e1;
  u32 tagLo, tagHi;
#define SET_TAG e1 = (((u64)(u32)__builtin_amdgcn_readfirstlane(ep.y) << 32) | (u32)__builtin_amdgcn_readfirstlane(ep.x)) + 1; tagLo = (u32)e1 << 16; tagHi = (u32)(e1 >> 16)
  if (!ctrl) {
    LANE_VARS;
    const size_t kb = ((size_t)h * SK + 512 * s + 64 * sw) * DH;
    const u16* kp = P.kc + kb + (n >> 2) * DH + 32 * (n & 3) + 8 * g;
    const u16* vp = P.vc + kb + 4 * g * DH + 8 * n;
    wg_barrier();
    UNR for (int i = 0; i < 16; ++i) { R[i] = ldw<NT_KV>(kp + i * 4 * DH); THROTTLE(WIN); }
    SET_TAG;
    ST(1);
    UNR for (int i = 0; i < 16; ++i) { R[16 + i] = ldw<NT_KV>(vp + ((i >> 2) * 16 + (i & 3)) * DH); THROTTLE(WIN); }
    ST(2);
  } else {
    LANE_VARS;
    if (tid == 0) L.cnt[0] = 0;
    u32x4 W[20];
    u32 cv[10], xv[5], xs = 0;
    u32x2 c2[10];
    const int line = b < 64 ? (b & 7) : 8 + ((b >> 2) & 1), sub = b < 64 ? (b >> 3) : (b & 3) + 4 * ((b >> 3) & 1);
    const int c0 = 64 * line + 8 * sub;
    if (isSaca) {
      UNR for (int i = 0; i < 5; ++i) W[i] = *(const u32x4*)(P.wm + (size_t)(160 * cw + 40 * g + 10 * (n >> 2) + 2 * i + ((n >> 1) & 1)) * NMOD + (n & 1) * D + c0);
      UNR for (int i = 0; i < 10; ++i) c2[i] = *(const u32x2*)(P.c + 160 * cw + 40 * g + 4 * i);
      UNR for (int i = 0; i < 5; ++i) xv[i] = ((const u32*)P.x)[lane + 64 * i];
      xs = ((const u32*)P.x)[(c0 + (lane & 7)) >> 1];
    }
    const int rq = 80 * s + 20 * cw + 5 * g;
    UNR for (int i = 0; i < 5; ++i) W[8 + i] = ldw<NT_W>(P.wq + (size_t)(rq + i) * 4096 + h * DH + 8 * n);
    if (cw == 3) {
      const u32x4 e = {EMPTY, EMPTY, EMPTY, EMPTY};
      st4(ws, (lane < 32 ? WS_QP : WS_OP - 512) + b * 512 + lane * 16, e);
      if (lane == 0) st4(ws, WS_ML + b * 64, e);
      if (isWo && lane < 16) st4(ws, WS_WOP + (wp * D + wc * 64) * 4 + lane * 16, e);
      if (isMlp) for (int i = lane; i < 160; i += 64) st4(ws, WS_DP + fi * (D * 4) + i * 16, e);
    }
    wg_barrier();
    SET_TAG;
    if (cw == 3) {
      asm volatile("s_waitcnt vmcnt(0)" ::: "memory");
      if (lane == 0) { u32x2 f = {tagLo, tagHi}; st2(ws, WS_FLAG + b * 8, f); }
    }
    if (isSaca) {
      f32x4 a4[4] = {zero4, zero4, zero4, zero4};
      UNR for (int k = 0; k < 10; ++k) {
        cv[k] = __builtin_amdgcn_perm(c2[k].y, c2[k].x, t ? 0x07060302u : 0x05040100u);
        const u32 w = pk2(silu_bf(bf2f(cv[k] & 0xFFFFu)), silu_bf(__uint_as_float(cv[k] & 0xFFFF0000u)));
        a4[(2 * k) / 5] = mfma(mkA(w, selL), W[(2 * k) % 5], a4[(2 * k) / 5]);
        a4[(2 * k + 1) / 5] = mfma(mkA(w, selH), W[(2 * k + 1) % 5], a4[(2 * k + 1) / 5]);
      }
      const f32x4 acc = (n & 8) ? ((n & 4) ? a4[3] : a4[2]) : ((n & 4) ? a4[1] : a4[0]);
      if ((g >> 1) == ((n >> 1) & 1)) *(f32x4*)&L.red[2 * cw + t][16 * ((n >> 1) & 3) + 8 * (n & 1) + 4 * (g & 1)] = acc;
      float mb = 0.f, sd = 1.f;
      if (cw == 0) {
        float v[10];
        UNR for (int i = 0; i < 5; ++i) { v[2 * i] = bf2f(xv[i] & 0xFFFFu); v[2 * i + 1] = __uint_as_float(xv[i] & 0xFFFF0000u); }
        ln_stats(v, mb, sd);
      }
      cbar(L, nb, lane);
      if (cw == 0) {
        float v = 0.f;
        UNR for (int k = 0; k < 8; ++k) v += L.red[k][lane];
        v = rbf(sum32(sum16(v)));
        const float hv = ln_mod(__uint_as_float((lane & 1) ? (xs & 0xFFFF0000u) : (xs << 16)), mb, sd, dpp<0x128>(v), v);
        const u32x2 gr = {f2bf(hv) | tagLo, tagHi};
        if (lane < 8) st2(ws, WS_H1 + (c0 + lane) * 8, gr);
      }
    }
    ST(1);
    {
      u32x2 hg[5];
      SPIN(NAP1, SPIN_CAP, const u32x2 f = ld2(ws, WS_FLAG + (8 * h + (lane & 7)) * 8); bool ok = true;
           UNR for (int i = 0; i < 5; ++i) hg[i] = ld2(ws, WS_H1 + (rq + i) * 8);
           UNR for (int i = 0; i < 5; ++i) ok = ok && TAGOK(hg[i]);
           ok = ok && TAGOK(f))
      ST(2);
      f32x4 acc = zero4;
      UNR for (int i = 0; i < 5; ++i) acc = mfma(mkA(hg[i].x, selL), W[8 + i], acc);
      if (g < 2) *(f32x4*)&L.ored[cw][0][8 * n + 4 * g] = acc;
      cbar(L, nb, lane);
    }
    if (cw == 0) {
      if (lane < 32) {
        f32x4 v = *(f32x4*)&L.ored[0][0][4 * lane];
        UNR for (int k = 1; k < 4; ++k) v += *(f32x4*)&L.ored[k][0][4 * lane];
        st4(ws, WS_QP + b * 512 + lane * 16, canon4(v));
      }
      u32x4 p[4];
      SPIN(2, SPIN_CAP, bool ok = true;
           UNR for (int k = 0; k < 4; ++k) p[k] = ld4(ws, WS_QP + h * 4096 + k * 1024 + lane * 16);
           UNR for (int k = 0; k < 4; ++k) ok = ok && full4(p[k]))
      const f32x4 v = (asf(p[0]) + asf(p[1])) + (asf(p[2]) + asf(p[3]));
      const u32x2 qq = {pk2(sum32(v.x), sum32(v.y)), pk2(sum32(v.z), sum32(v.w))};
      if (lane < 32) *(u32x2*)&L.q[2 * lane] = qq;
    }
    cbar(L, nb, lane);
    ST(3);
    if (isWo || isMlp) { SPIN(NAP1, SPIN_CAP, const u32x2 f = ld2(ws, WS_FLAG + (64 * cw + lane) * 8); const bool ok = TAGOK(f)) }
    if (isMod) {
      const int cm = 64 * (b - B_MOD);
      UNR for (int i = 0; i < 5; ++i) { const u32x2 v = *(const u32x2*)(P.c + r20 + 4 * i); cv[2 * i] = v.x; cv[2 * i + 1] = v.y; }
      UNR for (int i = 0; i < 20; ++i) { W[i] = ldw<NT_W>(P.wm + (size_t)(r20 + i) * NMOD + 1280 + cm + 8 * (n & 7)); THROTTLE(WINC); }
      f32x4 acc = zero4;
      UNR for (int i = 0; i < 10; ++i) {
        const u32 w = pk2(silu_bf(bf2f(cv[i] & 0xFFFFu)), silu_bf(__uint_as_float(cv[i] & 0xFFFF0000u)));
        acc = mfma(mkA(w, selL), W[2 * i], acc);
        acc = mfma(mkA(w, selH), W[2 * i + 1], acc);
      }
      if ((g >> 1) == t) *(f32x4*)&L.red[2 * cw + t][8 * (n & 7) + 4 * (g & 1)] = acc;
      cbar(L, nb, lane);
      if (cw == 0) {
        float v = 0.f;
        UNR for (int k = 0; k < 8; ++k) v += L.red[k][lane];
        const u32x2 gr = {f2bf(v) | tagLo, tagHi};
        st2(ws, WS_MOD + (cm + lane) * 8, gr);
      }
      cbar(L, nb, lane);
    }
    FORGET_R;
  }
#pragma unroll 1
  for (int pass = (ctrl && DRY) ? 0 : 1; pass < 2; ++pass) {
    const bool dry = pass == 0;
    const u32 cap = dry ? 1u : SPIN_CAP;
    if (!dry) {
      LANE_VARS;
      if (ctrl && isWo) {
        const u16* src = P.wo + (size_t)(128 * (4 * wp + cw) + 16 * (2 * g + t)) * D + 64 * wc + 8 * (n & 7);
        UNR for (int i = 0; i < 16; ++i) { R[i] = ldw<NT_W>(src + i * D); THROTTLE(WINC); }
      }
      if (ctrl && isMlp) {
        const u16* src = (((n >> 2) & 1) ? P.wu : P.wg) + (size_t)r20 * DF + f0 + 8 * (n & 3);
        UNR for (int i = 0; i < 20; ++i) { R[i] = ldw<0>(src + i * DF); THROTTLE(WINC); }
      }
      ST(13);
      wg_barrier();
      ST(4);
    }
    if (dry || !ctrl) {
      LANE_VARS;
      const int aw = wave & 7;
      float sc[4];
      const u32x4 aq = *(u32x4*)&L.q[16 * (n & 3) + 4 * g], z = {0u, 0u, 0u, 0u};
      u32x4 A[4];
      UNR for (int k = 0; k < 4; ++k) A[k] = (n >> 2) == k ? aq : z;
      UNR for (int G = 0; G < 4; ++G) {
        f32x4 acc = zero4;
        UNR for (int k = 0; k < 4; ++k) acc = mfma(A[k], R[4 * G + k], acc);
        const float v = (n & 2) ? ((n & 1) ? acc.w : acc.z) : ((n & 1) ? acc.y : acc.x);
        sc[G] = rbf(rbf(quad_sum(v)) * 0.08838834764831845f);
      }
      const float m = top_max(fmaxf(fmaxf(sc[0], sc[1]), fmaxf(sc[2], sc[3])));
      u32 pw[4];
      float l = 0.f;
      UNR for (int G = 0; G < 4; ++G) { const float p = __expf(sc[G] - m), hi = rbf(p); l += p; pw[G] = pk2(hi, p - hi); }
      l = top_sum(l);
      const u32x4 selP = t ? selH : selL;
      f32x4 acc = zero4;
      UNR for (int G = 0; G < 4; ++G) {
        acc = mfma(mkA(dppu<0x150>(pw[G]), selP), R[16 + 4 * G], acc);
        acc = mfma(mkA(dppu<0x154>(pw[G]), selP), R[17 + 4 * G], acc);
        acc = mfma(mkA(dppu<0x158>(pw[G]), selP), R[18 + 4 * G], acc);
        acc = mfma(mkA(dppu<0x15C>(pw[G]), selP), R[19 + 4 * G], acc);
      }
      *(f32x4*)&L.ored[aw][g >> 1][8 * n + 4 * (g & 1)] = acc;
      if (lane == 0) { L.ml[aw][0] = m; L.ml[aw][1] = l; }
    }
    if (!dry) { ST(14); wg_barrier(); ST(5); }
    LANE_VARS;
    if (wave == 0) {
      const float mw = L.ml[lane & 7][0], lw = L.ml[lane & 7][1];
      float m = fmaxf(mw, dpp<0xB1>(mw)); m = fmaxf(m, dpp<0x4E>(m)); m = fmaxf(m, dpp<0x141>(m));
      const float e = __expf(mw - m);
      float l = quad_sum(e * lw); l += dpp<0x141>(l);
      f32x4 o = zero4;
      UNR for (int k = 0; k < 8; ++k) o += __int_as_float(__builtin_amdgcn_readlane(__float_as_int(e), k)) * *(f32x4*)&L.ored[k][lane >> 5][4 * (lane & 31)];
      o.x = sum32(o.x); o.y = sum32(o.y); o.z = sum32(o.z); o.w = sum32(o.w);
      const u32x2 mlv = {canon(m), canon(l)};
      if (!dry && lane < 32) st4(ws, WS_OP + b * 512 + lane * 16, canon4(o));
      if (!dry && lane == 32) st2(ws, WS_ML + b * 64, mlv);
      ST(6);
    }
    if (ctrl && isWo) {
      const int hh = 4 * wp + cw;
      u32x4 p[4]; u32x2 mlv;
      SPIN(1, cap, mlv = ld2(ws, WS_ML + (hh * 8 + (lane & 7)) * 64);
           UNR for (int k = 0; k < 4; ++k) p[k] = ld4(ws, WS_OP + hh * 4096 + k * 1024 + lane * 16);
           bool ok = mlv.x != EMPTY && mlv.y != EMPTY;
           UNR for (int k = 0; k < 4; ++k) ok = ok && full4(p[k]))
      ST(7);
      const float mw = __uint_as_float(mlv.x), lw = __uint_as_float(mlv.y);
      float m = fmaxf(mw, dpp<0xB1>(mw)); m = fmaxf(m, dpp<0x4E>(m)); m = fmaxf(m, dpp<0x141>(m));
      float e = __expf(mw - m);
      float l = quad_sum(e * lw); l += dpp<0x141>(l);
      e = e / l;
      f32x4 o = zero4;
      UNR for (int k = 0; k < 4; ++k) {
        const float e0 = __int_as_float(__builtin_amdgcn_readlane(__float_as_int(e), 2 * k)), e1v = __int_as_float(__builtin_amdgcn_readlane(__float_as_int(e), 2 * k + 1));
        o += (lane < 32 ? e0 : e1v) * asf(p[k]);
      }
      const u32x2 ob = {pk2(sum32(o.x), sum32(o.y)), pk2(sum32(o.z), sum32(o.w))};
      if (lane < 32) *(u32x2*)&L.vec[wave][2 * lane] = ob;
      lds_wait();
      const u32x4 m0 = *(u32x4*)&L.vec[wave][8 * (2 * g + t)], m1 = *(u32x4*)&L.vec[wave][8 * (2 * g + t) + 4];
      f32x4 acc = zero4;
      UNR for (int i = 0; i < 8; ++i) {
        const u32 w = i < 4 ? m0[i & 3] : m1[i & 3];
        acc = mfma(mkA(w, selL), R[2 * i], acc);
        acc = mfma(mkA(w, selH), R[2 * i + 1], acc);
      }
      if ((g >> 1) == t) *(f32x4*)&L.red[2 * cw + t][8 * (n & 7) + 4 * (g & 1)] = acc;
      cbar(L, nb, lane);
      if (cw == 0) {
        float v = 0.f;
        UNR for (int k = 0; k < 8; ++k) v += L.red[k][lane];
        if (!dry) st1(ws, WS_WOP + (wp * D + 64 * wc + lane) * 4, canon(v));
      }
      ST(8);
    }
    if (isMlp) {
      const bool dn = dry || (!ctrl && sw < 5);
      const int dw = dry ? cw : sw;
      if (!dry && dn) {
        __builtin_amdgcn_s_sleep(6);
        const u16* src = P.wd + (size_t)(f0 + 8 * g) * D + 128 * sw + 8 * n;
        UNR for (int i = 0; i < 8; ++i) R[i] = ldw<NT_W>(src + i * D);
      }
      if (dry || sw >= 5) {
        const int v = dry ? 0 : sw - 5;
        u32x2 mg[10];
        SPIN(2, cap, bool ok = true;
             UNR for (int i = 0; i < 10; ++i) mg[i] = ld2(ws, WS_MOD + ((v + 1) * D + lane + 64 * i) * 8);
             UNR for (int i = 0; i < 10; ++i) ok = ok && TAGOK(mg[i]))
        UNR for (int i = 0; i < 10; ++i) L.modv[v][lane + 64 * i] = bf2f(mg[i].x & 0xFFFFu);
      }
      if (dn) {
        u32x4 p[4], g0, g1;
        const int col = 128 * dw + 4 * (lane & 31);
        const u32x2 xx = *(const u32x2*)(P.x + col);
        SPIN(1, cap, g0 = ld4(ws, WS_MOD + col * 8); g1 = ld4(ws, WS_MOD + col * 8 + 16);
             UNR for (int k = 0; k < 4; ++k) p[k] = ld4(ws, WS_WOP + ((2 * k + (lane >> 5)) * D + col) * 4);
             bool ok = TAGOK(g0.xy) && TAGOK(g0.zw) && TAGOK(g1.xy) && TAGOK(g1.zw);
             UNR for (int k = 0; k < 4; ++k) ok = ok && full4(p[k]))
        ST(7);
        const f32x4 a = (asf(p[0]) + asf(p[1])) + (asf(p[2]) + asf(p[3]));
        f32x4 x2;
        x2.x = rbf(bf2f(xx.x) + rbf(bf2f(g0.x) * rbf(sum32(a.x))));
        x2.y = rbf(__uint_as_float(xx.x & 0xFFFF0000u) + rbf(bf2f(g0.z) * rbf(sum32(a.y))));
        x2.z = rbf(bf2f(xx.y) + rbf(bf2f(g1.x) * rbf(sum32(a.z))));
        x2.w = rbf(__uint_as_float(xx.y & 0xFFFF0000u) + rbf(bf2f(g1.z) * rbf(sum32(a.w))));
        if (lane < 32) *(f32x4*)&L.x2[col] = x2;
      }
      if (!dry) wg_barrier();
      if (ctrl) {
        ST(9);
        float v[10], mb, sd;
        UNR for (int i = 0; i < 10; ++i) v[i] = L.x2[lane + 64 * i];
        ln_stats(v, mb, sd);
        UNR for (int k = 0; k < 2; ++k) {
          const int pi = lane + 64 * k, r = 160 * cw + 2 * pi;
          if (pi < 80) L.vec[wave][pi] = pk2(ln_mod(L.x2[r], mb, sd, L.modv[1][r], L.modv[0][r]), ln_mod(L.x2[r + 1], mb, sd, L.modv[1][r + 1], L.modv[0][r + 1]));
        }
        lds_wait();
        f32x4 acc = zero4;
        UNR for (int i = 0; i < 5; ++i) {
          const u32x2 w = *(u32x2*)&L.vec[wave][10 * (2 * g + t) + 2 * i];
          acc = mfma(mkA(w.x, selL), R[4 * i], acc);
          acc = mfma(mkA(w.x, selH), R[4 * i + 1], acc);
          acc = mfma(mkA(w.y, selL), R[4 * i + 2], acc);
          acc = mfma(mkA(w.y, selH), R[4 * i + 3], acc);
        }
        if ((g >> 1) == t) *(f32x4*)&L.red[2 * cw + t][8 * (n & 7) + 4 * (g & 1)] = acc;
      }
      if (!dry) wg_barrier();
      if (dn) {
        ST(15);
        float v = 0.f;
        UNR for (int k = 0; k < 8; ++k) v += L.red[k][lane];
        v = rbf(v);
        auto r = __builtin_amdgcn_permlane32_swap(__float_as_uint(v), __float_as_uint(v), false, false);
        const float gv = __uint_as_float(r[0]), uv = __uint_as_float(r[1]);
        const float in = 0.7978845608028654f * (gv + 0.044715f * gv * gv * gv);
        const float th = 1.f - 2.f / (__expf(2.f * in) + 1.f);
        const u32 hb = f2bf(rbf(0.5f * gv * (1.f + th)) * uv);
        if (lane < 32) ((u16*)L.vec[wave])[lane] = (u16)hb;
        lds_wait();
        const u32x4 mm = *(u32x4*)&L.vec[wave][4 * g];
        f32x4 y = zero4;
        UNR for (int i = 0; i < 4; ++i) { y = mfma(mkA(mm[i], selL), R[2 * i], y); y = mfma(mkA(mm[i], selH), R[2 * i + 1], y); }
        if (!dry && g < 2) st4(ws, WS_DP + (fi * D + 128 * dw + 8 * n + 4 * g) * 4, canon4(y));
        ST(10);
      }
      if (wave == 0 && fi < N_FIN) {
        u32x4 p[4];
        const int pp = lane >> 2, col = 16 * fi + 4 * (lane & 3);
        const bool has3 = pp + 48 < N_MLP;
        SPIN(1, cap, bool ok = true;
             UNR for (int k = 0; k < 4; ++k) p[k] = ld4(ws, WS_DP + col * 4 + (k < 3 || has3 ? pp + 16 * k : pp) * (D * 4));
             UNR for (int k = 0; k < 4; ++k) ok = ok && full4(p[k]))
        ST(11);
        f32x4 y = asf(p[0]) + asf(p[1]) + asf(p[2]);
        if (has3) y += asf(p[3]);
        const f32x4 x2 = *(f32x4*)&L.x2[col], gb = *(f32x4*)&L.modv[2][col];
        const u32x2 o = {pk2(x2.x + rbf(gb.x * rbf(top_sum(y.x))), x2.y + rbf(gb.y * rbf(top_sum(y.y)))),
                         pk2(x2.z + rbf(gb.z * rbf(top_sum(y.z))), x2.w + rbf(gb.w * rbf(top_sum(y.w))))};
        if (!dry && lane < 4) *(u32x2*)(P.out + col) = o;
        if (!dry && fi == 0 && lane == 0) { u32x2 e = {(u32)e1, (u32)(e1 >> 32)}; asm volatile("" : "+s"(e)); st2(ws, WS_EPOCH, e); }
        ST(12);
      }
    }
    FORGET_R;
  }
}

extern "C" void kernel_launch(void* const* d_in, const int* in_sizes, int n_in, void* d_out, int out_size,
                              void* d_ws, size_t ws_size, hipStream_t stream) {
  Params p;
  p.x = (const u16*)d_in[0]; p.kc = (const u16*)d_in[1]; p.vc = (const u16*)d_in[2];
  p.wq = (const u16*)d_in[3]; p.wo = (const u16*)d_in[4];
  p.wg = (const u16*)d_in[6]; p.wu = (const u16*)d_in[7]; p.wd = (const u16*)d_in[8];
  p.c = (const u16*)d_in[9]; p.wm = (const u16*)d_in[10];
  p.out = (u16*)d_out; p.ws = (char*)d_ws;
  adaln_block_kernel<<<256, 768, 0, stream>>>(p);
}
```

```cpp
#include <hip/hip_runtime.h>
#include <hip/hip_bf16.h>

typedef unsigned int u32;
typedef unsigned long long u64;
typedef unsigned short u16;
typedef __attribute__((ext_vector_type(4))) u32 u32x4;
typedef __attribute__((ext_vector_type(2))) u32 u32x2;
typedef __attribute__((ext_vector_type(4))) float f32x4;
typedef __attribute__((ext_vector_type(8))) __bf16 bf16x8;
typedef __attribute__((ext_vector_type(2))) __bf16 bf16x2;
typedef __attribute__((ext_vector_type(2))) float f32x2;
#define DEV __device__ __forceinline__

#define WIN 2
#define VQ2 2
#define PF 0
#define CODE_LINES 156
#define WINM 3
#define WINH 4
#define VQ 8
#define NAP1 16
#define NT_G 1
#define WINC 4
#define PH1 6
#define NAP0 64
#define NT_KV 1
#define NT_W 1

constexpr int D = 640, DH = 128, SK = 4096, DF = 1728, NMOD = 3840;
constexpr int N_SACA = 80, B_MOD = 80, B_WO = 120, B_MLP = 200, N_MLP = 54, N_FIN = 40;
constexpr u32 WS_EPOCH = 0, WS_FLAG = 2048, WS_MOD = 4096, WS_H1 = 24576, WS_QP = 65536, WS_OP = 196608,
              WS_ML = 327680, WS_WOP = 344064, WS_DP = 393216, WS_BYTES = 655360, WS_STAMP = 1 << 20;
constexpr u32 EMPTY = 0xFFFFFFFFu;
constexpr u32 SPIN_CAP = 1u << 22;
constexpr int SC1 = 16;

struct Params {
  const u16 *x, *kc, *vc, *wq, *wo, *wg, *wu, *wd, *c, *wm;
  u16* out;
  char* ws;
};

DEV float bf2f(u32 v) { return __uint_as_float(v << 16); }
DEV u32 pk2(float lo, float hi) { f32x2 f = {lo, hi}; return __builtin_bit_cast(u32, __builtin_convertvector(f, bf16x2)); }
DEV u32 f2bf(float f) { return pk2(f, 0.f) & 0xFFFFu; }
DEV float rbf(float f) { return __uint_as_float(pk2(0.f, f) & 0xFFFF0000u); }
DEV u32 canon(float f) { return min(__float_as_uint(f), EMPTY - 1u); }
template <int C> DEV float dpp(float v) { return __int_as_float(__builtin_amdgcn_update_dpp(0, __float_as_int(v), C, 0xF, 0xF, true)); }
template <int C> DEV u32 dppu(u32 v) { return (u32)__builtin_amdgcn_update_dpp(0, (int)v, C, 0xF, 0xF, true); }
DEV float sum16(float v) { u32 u = __float_as_uint(v); auto r = __builtin_amdgcn_permlane16_swap(u, u, false, false); return __uint_as_float(r[0]) + __uint_as_float(r[1]); }
DEV float sum32(float v) { u32 u = __float_as_uint(v); auto r = __builtin_amdgcn_permlane32_swap(u, u, false, false); return __uint_as_float(r[0]) + __uint_as_float(r[1]); }
DEV float max16(float v) { u32 u = __float_as_uint(v); auto r = __builtin_amdgcn_permlane16_swap(u, u, false, false); return fmaxf(__uint_as_float(r[0]), __uint_as_float(r[1])); }
DEV float max32(float v) { u32 u = __float_as_uint(v); auto r = __builtin_amdgcn_permlane32_swap(u, u, false, false); return fmaxf(__uint_as_float(r[0]), __uint_as_float(r[1])); }
DEV float quad_sum(float v) { v += dpp<0xB1>(v); v += dpp<0x4E>(v); return v; }
DEV float top_sum(float v) { v += dpp<0x124>(v); v += dpp<0x128>(v); return sum32(sum16(v)); }
DEV float top_max(float v) { v = fmaxf(v, dpp<0x124>(v)); v = fmaxf(v, dpp<0x128>(v)); return max32(max16(v)); }
DEV float wave_sum(float v) { return top_sum(quad_sum(v)); }
DEV f32x4 mfma(u32x4 a, u32x4 b, f32x4 c) { return __builtin_amdgcn_mfma_f32_16x16x32_bf16(__builtin_bit_cast(bf16x8, a), __builtin_bit_cast(bf16x8, b), c, 0, 0, 0); }
DEV u32x4 mkA(u32 w, u32x4 s) { u32x4 a; a.x = __builtin_amdgcn_perm(w, w, s.x); a.y = __builtin_amdgcn_perm(w, w, s.y); a.z = __builtin_amdgcn_perm(w, w, s.z); a.w = __builtin_amdgcn_perm(w, w, s.w); return a; }
DEV u32x4 mksel(int j, int hi) { u32 nz = (j & 1) ? (hi ? 0x03020c0cu : 0x01000c0cu) : (hi ? 0x0c0c0302u : 0x0c0c0100u); int q = j >> 1; u32 z = 0x0c0c0c0cu;
  u32x4 s = {q == 0 ? nz : z, q == 1 ? nz : z, q == 2 ? nz : z, q == 3 ? nz : z}; return s; }
template <int NT> DEV u32x4 ldw(const u16* p) { if (NT) return __builtin_nontemporal_load((const u32x4*)p); else return *(const u32x4*)p; }
DEV void fence() { asm volatile("" ::: "memory"); }
DEV void wg_barrier() { asm volatile("s_waitcnt lgkmcnt(0)\n\ts_barrier" ::: "memory"); }
DEV void lds_wait() { asm volatile("s_waitcnt lgkmcnt(0)" ::: "memory"); }
typedef __amdgpu_buffer_rsrc_t rsrc_t;
DEV u32x4 ld4(rsrc_t r, u32 off) { return __builtin_amdgcn_raw_buffer_load_b128(r, off, 0, SC1); }
DEV u32x2 ld2(rsrc_t r, u32 off) { return __builtin_amdgcn_raw_buffer_load_b64(r, off, 0, SC1); }
DEV void st4(rsrc_t r, u32 off, u32x4 v) { __builtin_amdgcn_raw_buffer_store_b128(v, r, off, 0, SC1); }
DEV void st2(rsrc_t r, u32 off, u32x2 v) { __builtin_amdgcn_raw_buffer_store_b64(v, r, off, 0, SC1); }
DEV void st1(rsrc_t r, u32 off, u32 v) { __builtin_amdgcn_raw_buffer_store_b32(v, r, off, 0, SC1); }
DEV u32 mx3(u32 a, u32 b, u32 c) { return max(max(a, b), c); }
DEV u32 mx4(u32x4 v) { return max(mx3(v.x, v.y, v.z), v.w); }
DEV bool full16(const u32x4 (&p)[4], u32 x) { return mx3(mx3(mx3(p[0].x, p[0].y, p[0].z), mx3(p[0].w, p[1].x, p[1].y), mx3(p[1].z, p[1].w, p[2].x)), mx3(mx3(p[2].y, p[2].z, p[2].w), mx3(p[3].x, p[3].y, p[3].z), p[3].w), x) != EMPTY; }
DEV f32x4 asf(u32x4 v) { return __builtin_bit_cast(f32x4, v); }
DEV u32x4 canon4(f32x4 v) { u32x4 r = {canon(v.x), canon(v.y), canon(v.z), canon(v.w)}; return r; }
#define SPIN(nap, cap, cond) _Pragma("unroll 1") for (u32 spin_ = 0; spin_ < (cap); ++spin_) { fence(); cond; if (__all(ok)) break; __builtin_amdgcn_s_sleep(nap); }
#define TAGBAD(v) (((((v).x ^ tagLo) & 0xFFu) | ((v).y ^ tagHi)))
#define UNR _Pragma("unroll")
#define THROTTLE(w) asm volatile("s_waitcnt vmcnt(%0)" ::"n"(w) : "memory")
DEV int pairmap(int i, int n) { return i < (n & ~15) ? ((i & ~15) | ((i & 7) << 1) | ((i >> 3) & 1)) : i; }
DEV float silu_bf(float v) { return v * __builtin_amdgcn_rcpf(1.f + __expf(-v)); }
#define ST(k)

struct __attribute__((aligned(16))) Lds {
  float red[8][64];
  float ored[8][128];
  float x2[D];
  float modv[3][D];
  u32 q[64];
  u32 vec[12][80];
  float mw[8], lw[8], st[8][2];
  u32 cnt[4];
};

DEV void cbar(Lds& L, u32& target, int lane) {
  target += 4;
  lds_wait();
  if (lane == 0) __hip_atomic_fetch_add(&L.cnt[0], 1u, __ATOMIC_RELAXED, __HIP_MEMORY_SCOPE_WORKGROUP);
#pragma unroll 1
  for (u32 s = 0; s < SPIN_CAP; ++s) { fence(); if (__hip_atomic_load(&L.cnt[0], __ATOMIC_RELAXED, __HIP_MEMORY_SCOPE_WORKGROUP) >= target) break; }
  fence();
}
DEV void ln_fin(float s1, float s2, float& mean_b, float& rsd) {
  const float m = s1 * (1.f / D), var = rbf(fmaxf(s2 * (1.f / D) - m * m, 0.f));
  mean_b = rbf(m);
  rsd = __builtin_amdgcn_rcpf(rbf(__builtin_amdgcn_sqrtf(rbf(var + 1e-5f))));
}
DEV float ln_mod(float x, float mean_b, float rsd, float sc, float sh) { return rbf(rbf(rbf(x - mean_b) * rsd) * rbf(1.f + sc)) + sh; }
DEV float lo(u32 v) { return __uint_as_float(v << 16); }
DEV float hi(u32 v) { return __uint_as_float(v & 0xFFFF0000u); }
DEV float gval(u32 v) { return __uint_as_float(v & 0xFFFFFF00u); }
DEV float sum8(const Lds& L, int lane) { return ((L.red[0][lane] + L.red[1][lane]) + (L.red[2][lane] + L.red[3][lane])) + ((L.red[4][lane] + L.red[5][lane]) + (L.red[6][lane] + L.red[7][lane])); }

__global__ __launch_bounds__(768, 3) void adaln_block_kernel(Params P) {
  __shared__ Lds L;
  const int tid = threadIdx.x, lane = tid & 63, b = blockIdx.x;
  const int wave = __builtin_amdgcn_readfirstlane(tid >> 6);
  const int g = lane >> 4, n = lane & 15, t = n >> 3;
  const int h = b >> 3, s = b & 7;
  const bool isSaca = b < N_SACA, isMod = b >= B_MOD && b < B_WO, isWo = b >= B_WO && b < B_MLP, isMlp = b >= B_MLP && b < B_MLP + N_MLP;
  const rsrc_t ws = __builtin_amdgcn_make_buffer_rsrc(P.ws, 0, WS_BYTES, 0x00020000);
  const f32x4 zero4 = {0.f, 0.f, 0.f, 0.f};
  const int fi = b - B_MLP, f0 = 32 * pairmap(fi, N_MLP);
  ST(0);
  const u64 rt0 = __builtin_amdgcn_s_memrealtime();
  u64 pc0;
  asm volatile("s_getpc_b64 %0" : "=s"(pc0));
  const u32x2 ep = ld2(ws, WS_EPOCH);
#define SET_TAG const u64 e1 = (((u64)(u32)__builtin_amdgcn_readfirstlane(ep.y) << 32) | (u32)__builtin_amdgcn_readfirstlane(ep.x)) + 1; const u32 tagLo = (u32)e1 & 0xFFu, tagHi = (u32)(e1 >> 8)
  if (wave < 4) {
    const int cw = wave, r8 = 2 * g + t, r20 = 160 * cw + 20 * r8;
    const int wp = (b - B_WO) / 10, wc = (b - B_WO) % 10;
    if (tid == 0) L.cnt[0] = 0;
    u32 nb = 0;
    u32x4 W[20], WQ[5];
    u32 cv[10], xv[5];
    const int line = b % 20, kq = b / 20, rs = 160 * kq + 40 * cw + 2 * r8;
    if (isSaca) {
      UNR for (int i = 0; i < 5; ++i) W[i] = ldw<NT_W>(P.wm + (size_t)(i < 4 ? rs + 16 * (i >> 1) + (i & 1) : rs + 32 - r8) * NMOD + 64 * line + 8 * (n & 7));
      cv[0] = ((const u32*)P.c)[rs >> 1]; cv[1] = ((const u32*)P.c)[(rs >> 1) + 8]; cv[2] = ((const u32*)P.c)[(rs + 32 - r8) >> 1];
    }
    const int rq = 80 * s + 20 * cw + 5 * g, mr = rq + n % 5;
    UNR for (int i = 0; i < 5; ++i) xv[i] = ((const u32*)P.x)[lane + 64 * i];
    const u32 xs = ((const u32*)P.x)[mr >> 1];
    UNR for (int i = 0; i < 5; ++i) WQ[i] = ldw<NT_W>(P.wq + (size_t)(rq + i) * 4096 + h * DH + 8 * n);
    if (cw == 3) {
      const u32x4 e = {EMPTY, EMPTY, EMPTY, EMPTY};
      st4(ws, (lane < 32 ? WS_QP : WS_OP - 512) + b * 512 + lane * 16, e);
      if (lane == 0) st4(ws, WS_ML + b * 64, e);
      if (isWo && lane < 16) st4(ws, WS_WOP + (wp * D + wc * 64) * 4 + lane * 16, e);
      if (isMlp) for (int i = lane; i < 160; i += 64) st4(ws, WS_DP + fi * (D * 4) + i * 16, e);
    }
    wg_barrier();
    SET_TAG;
    const u32x4 selL = mksel(n & 7, 0), selH = mksel(n & 7, 1);
    if (isSaca) {
      const u32 w0 = pk2(silu_bf(lo(cv[0])), silu_bf(hi(cv[0]))), w1 = pk2(silu_bf(lo(cv[1])), silu_bf(hi(cv[1])));
      const u32 w2 = pk2(silu_bf(t ? hi(cv[2]) : lo(cv[2])), 0.f);
      f32x4 acc = mfma(mkA(w0, selL), W[0], zero4);
      acc = mfma(mkA(w0, selH), W[1], acc);
      acc = mfma(mkA(w1, selL), W[2], acc);
      acc = mfma(mkA(w1, selH), W[3], acc);
      acc = mfma(mkA(w2, selL), W[4], acc);
      if ((g >> 1) == t) *(f32x4*)&L.red[2 * cw + t][8 * (n & 7) + 4 * (g & 1)] = acc;
      cbar(L, nb, lane);
      if (cw == 0) { const u32x2 gr = {((__float_as_uint(sum8(L, lane)) + 0x80u) & 0xFFFFFF00u) | tagLo, tagHi}; st2(ws, WS_H1 + (64 * line + lane) * 32 + kq * 8, gr); }
    }
    if (cw == 3) {
      asm volatile("s_waitcnt vmcnt(0)" ::: "memory");
      if (lane == 0) { const u32x2 f = {tagLo, tagHi}; st2(ws, WS_FLAG + b * 8, f); }
    }
    float mb, rsd;
    {
      float s1 = 0.f, s2 = 0.f;
      UNR for (int i = 0; i < 5; ++i) { const float a = lo(xv[i]), c = hi(xv[i]); s1 += a + c; s2 += a * a + c * c; }
      ln_fin(wave_sum(s1), wave_sum(s2), mb, rsd);
    }
    const bool role = isWo || isMlp || isMod;
    const u16* wsrc = isWo ? P.wo + (size_t)(128 * (4 * wp + cw) + 16 * r8) * D + 64 * wc + 8 * (n & 7)
                    : isMlp ? (((n >> 2) & 1) ? P.wu : P.wg) + (size_t)r20 * DF + f0 + 8 * (n & 3)
                            : P.wm + (size_t)r20 * NMOD + 1280 + 64 * (b - B_MOD) + 8 * (n & 7);
    const int wstr = isWo ? D : isMlp ? DF : NMOD;
    ST(1);
    if (role) {
      if (isMod) { UNR for (int i = 0; i < 5; ++i) { const u32x2 v = *(const u32x2*)(P.c + r20 + 4 * i); cv[2 * i] = v.x; cv[2 * i + 1] = v.y; } }
      UNR for (int i = 0; i < PH1; ++i) { W[i] = ldw<NT_W>(wsrc + i * wstr); THROTTLE(WINC); }
    } else if (!isSaca) __builtin_amdgcn_s_sleep(NAP0);
    {
      u32x4 p0, p1;
      const u32 off = WS_H1 + (mr + (n >= 5 && n < 10 ? D : 0)) * 32;
      SPIN(NAP1, SPIN_CAP, const u32x2 f = ld2(ws, cw == 3 ? WS_FLAG + (8 * h + (lane & 7)) * 8 : off); p0 = ld4(ws, off); p1 = ld4(ws, off + 16);
           const bool ok = (TAGBAD(f) | TAGBAD(p0.xy) | TAGBAD(p0.zw) | TAGBAD(p1.xy) | TAGBAD(p1.zw)) == 0)
      ST(2);
      const float v = rbf((gval(p0.x) + gval(p0.z)) + (gval(p1.x) + gval(p1.z)));
      const u32 hb = pk2(ln_mod((mr & 1) ? hi(xs) : lo(xs), mb, rsd, dpp<0x105>(v), v), 0.f);
      f32x4 acc = mfma(mkA(dppu<0x150>(hb), selL), WQ[0], zero4);
      acc = mfma(mkA(dppu<0x151>(hb), selL), WQ[1], acc);
      acc = mfma(mkA(dppu<0x152>(hb), selL), WQ[2], acc);
      acc = mfma(mkA(dppu<0x153>(hb), selL), WQ[3], acc);
      acc = mfma(mkA(dppu<0x154>(hb), selL), WQ[4], acc);
      if (g < 2) *(f32x4*)&L.ored[cw][8 * n + 4 * g] = acc;
      cbar(L, nb, lane);
    }
    if (cw == 0) {
      const int l4 = 4 * (lane & 31);
      const f32x4 v0 = (*(f32x4*)&L.ored[0][l4] + *(f32x4*)&L.ored[1][l4]) + (*(f32x4*)&L.ored[2][l4] + *(f32x4*)&L.ored[3][l4]);
      if (lane < 32) st4(ws, WS_QP + b * 512 + lane * 16, canon4(v0));
      u32x4 p[4];
      SPIN(2, SPIN_CAP, UNR for (int k = 0; k < 4; ++k) p[k] = ld4(ws, WS_QP + h * 4096 + k * 1024 + lane * 16); const bool ok = full16(p, 0))
      const f32x4 v = (asf(p[0]) + asf(p[1])) + (asf(p[2]) + asf(p[3]));
      const u32x2 qq = {pk2(sum32(v.x), sum32(v.y)), pk2(sum32(v.z), sum32(v.w))};
      if (lane < 32) *(u32x2*)&L.q[2 * lane] = qq;
    }
    ST(3);
    if (cw == 3 && (isWo || isMlp)) {
      SPIN(NAP1, SPIN_CAP, const u32x4 f0v = ld4(ws, WS_FLAG + lane * 32); const u32x4 f1v = ld4(ws, WS_FLAG + lane * 32 + 16);
           const bool ok = (TAGBAD(f0v.xy) | TAGBAD(f0v.zw) | TAGBAD(f1v.xy) | TAGBAD(f1v.zw)) == 0)
    }
    if (cw == 0) { wg_barrier(); ST(4); wg_barrier(); }
    if (role) { UNR for (int i = PH1; i < 20; ++i) if (i < 16 || !isWo) { W[i] = ldw<NT_W>(wsrc + i * wstr); THROTTLE(WINC); } }
    ST(13);
    if (cw != 0) { wg_barrier(); ST(4); wg_barrier(); }
    if (isMod) {
      f32x4 acc = zero4;
      UNR for (int i = 0; i < 10; ++i) {
        const u32 w = pk2(silu_bf(lo(cv[i])), silu_bf(hi(cv[i])));
        acc = mfma(mkA(w, selL), W[2 * i], acc);
        acc = mfma(mkA(w, selH), W[2 * i + 1], acc);
      }
      if ((g >> 1) == t) *(f32x4*)&L.red[2 * cw + t][8 * (n & 7) + 4 * (g & 1)] = acc;
      cbar(L, nb, lane);
      if (cw == 1) { const u32x2 gr = {__float_as_uint(rbf(sum8(L, lane))) | tagLo, tagHi}; st2(ws, WS_MOD + (64 * (b - B_MOD) + lane) * 8, gr); }
    }
    const float mwg = fmaxf(fmaxf(fmaxf(L.mw[0], L.mw[1]), fmaxf(L.mw[2], L.mw[3])), fmaxf(fmaxf(L.mw[4], L.mw[5]), fmaxf(L.mw[6], L.mw[7])));
    wg_barrier();
    ST(5);
    if (cw == 0) {
      const int l4 = 4 * (lane & 31), k0 = 4 * (lane >> 5);
      f32x4 o = (*(f32x4*)&L.ored[k0][l4] + *(f32x4*)&L.ored[k0 + 1][l4]) + (*(f32x4*)&L.ored[k0 + 2][l4] + *(f32x4*)&L.ored[k0 + 3][l4]);
      const f32x4 la = *(f32x4*)&L.lw[0] + *(f32x4*)&L.lw[4];
      o.x = sum32(o.x); o.y = sum32(o.y); o.z = sum32(o.z); o.w = sum32(o.w);
      const u32x2 mlv = {canon(mwg), canon((la.x + la.y) + (la.z + la.w))};
      if (lane < 32) st4(ws, WS_OP + b * 512 + lane * 16, canon4(o));
      if (lane == 32) st2(ws, WS_ML + b * 64, mlv);
      ST(6);
    }
    if (isWo) {
      const int hh = 4 * wp + cw;
      u32x4 p[4]; u32x2 mlv;
      SPIN(1, SPIN_CAP, mlv = ld2(ws, WS_ML + (hh * 8 + (lane & 7)) * 64);
           UNR for (int k = 0; k < 4; ++k) p[k] = ld4(ws, WS_OP + hh * 4096 + k * 1024 + lane * 16);
           const bool ok = full16(p, max(mlv.x, mlv.y)))
      ST(7);
      const float mw = __uint_as_float(mlv.x), lw = __uint_as_float(mlv.y);
      float m = fmaxf(mw, dpp<0xB1>(mw)); m = fmaxf(m, dpp<0x4E>(m)); m = fmaxf(m, dpp<0x141>(m));
      float e = __expf(mw - m);
      float l = quad_sum(e * lw); l += dpp<0x141>(l);
      e *= __builtin_amdgcn_rcpf(l);
      e = lane < 32 ? e : dpp<0xB1>(e);
      const f32x4 o = (dpp<0x150>(e) * asf(p[0]) + dpp<0x152>(e) * asf(p[1])) + (dpp<0x154>(e) * asf(p[2]) + dpp<0x156>(e) * asf(p[3]));
      const u32x2 ob = {pk2(sum32(o.x), sum32(o.y)), pk2(sum32(o.z), sum32(o.w))};
      if (lane < 32) *(u32x2*)&L.vec[wave][2 * lane] = ob;
      lds_wait();
      const u32x4 m0 = *(u32x4*)&L.vec[wave][8 * r8], m1 = *(u32x4*)&L.vec[wave][8 * r8 + 4];
      f32x4 acc = zero4;
      UNR for (int i = 0; i < 8; ++i) {
        const u32 w = i < 4 ? m0[i & 3] : m1[i & 3];
        acc = mfma(mkA(w, selL), W[2 * i], acc);
        acc = mfma(mkA(w, selH), W[2 * i + 1], acc);
      }
      if ((g >> 1) == t) *(f32x4*)&L.red[2 * cw + t][8 * (n & 7) + 4 * (g & 1)] = acc;
      cbar(L, nb, lane);
      if (cw == 0) st1(ws, WS_WOP + (wp * D + 64 * wc + lane) * 4, canon(sum8(L, lane)));
      ST(8);
    }
    if (isMlp) {
      wg_barrier();
      ST(9);
      const int r = 160 * cw + 4 * (lane < 40 ? lane : 0);
      const f32x4 s01 = *(f32x4*)&L.st[0][0], s23 = *(f32x4*)&L.st[2][0], x2 = *(f32x4*)&L.x2[r], sb = *(f32x4*)&L.modv[0][r], cb = *(f32x4*)&L.modv[1][r];
      ln_fin((s01.x + s01.z) + (s23.x + s23.z) + L.st[4][0], (s01.y + s01.w) + (s23.y + s23.w) + L.st[4][1], mb, rsd);
      const u32x2 hb = {pk2(ln_mod(x2.x, mb, rsd, cb.x, sb.x), ln_mod(x2.y, mb, rsd, cb.y, sb.y)), pk2(ln_mod(x2.z, mb, rsd, cb.z, sb.z), ln_mod(x2.w, mb, rsd, cb.w, sb.w))};
      if (lane < 40) *(u32x2*)&L.vec[wave][2 * lane] = hb;
      lds_wait();
      f32x4 acc = zero4;
      UNR for (int i = 0; i < 5; ++i) {
        const u32x2 w = *(u32x2*)&L.vec[wave][10 * r8 + 2 * i];
        acc = mfma(mkA(w.x, selL), W[4 * i], acc);
        acc = mfma(mkA(w.x, selH), W[4 * i + 1], acc);
        acc = mfma(mkA(w.y, selL), W[4 * i + 2], acc);
        acc = mfma(mkA(w.y, selH), W[4 * i + 3], acc);
      }
      if ((g >> 1) == t) *(f32x4*)&L.red[2 * cw + t][8 * (n & 7) + 4 * (g & 1)] = acc;
      wg_barrier();
      if (cw == 0 && fi < N_FIN) {
        u32x4 p[4];
        const int pp = lane >> 2, col = 16 * fi + 4 * (lane & 3);
        const bool has3 = pp + 48 < N_MLP;
        const f32x4 xo = *(f32x4*)&L.x2[col], gb = *(f32x4*)&L.modv[2][col];
        SPIN(1, SPIN_CAP, UNR for (int k = 0; k < 4; ++k) p[k] = ld4(ws, WS_DP + col * 4 + (k < 3 || has3 ? pp + 16 * k : pp) * (D * 4)); const bool ok = full16(p, 0))
        ST(11);
        const f32x4 y = (asf(p[0]) + asf(p[1])) + (asf(p[2]) + (has3 ? asf(p[3]) : zero4));
        const u32x2 o = {pk2(xo.x + rbf(gb.x * rbf(top_sum(y.x))), xo.y + rbf(gb.y * rbf(top_sum(y.y)))),
                         pk2(xo.z + rbf(gb.z * rbf(top_sum(y.z))), xo.w + rbf(gb.w * rbf(top_sum(y.w))))};
        { const u64 c0_ = __builtin_amdgcn_s_memtime(); while (__builtin_amdgcn_s_memtime() - c0_ < 24000) __builtin_amdgcn_s_sleep(1); }
        if (lane < 4) *(u32x2*)(P.out + col) = o;
        if (fi == 0 && lane == 0) { const u32x2 e = {(u32)e1, (u32)(e1 >> 32)}; st2(ws, WS_EPOCH, e); }
        ST(12);
      }
    }
  } else {
    const int sw = wave - 4;
    const size_t kb = ((size_t)h * SK + 512 * s + 64 * sw) * DH;
    const u16* kp = P.kc + kb + (n >> 2) * DH + 32 * (n & 3) + 8 * g;
    const u16* vp = P.vc + kb + 4 * g * DH + 8 * n;
    u32x4 K[16], V[16];
    u32 pf = 0;
    if (PF && sw < 3 && lane + 64 * sw < CODE_LINES) pf = *(const __attribute__((address_space(1))) u32*)(((u64)pc0 & ~3ull) + (lane + 64 * sw) * 128);
    wg_barrier();
    #define THR_S do { if (isMlp) THROTTLE(WINH); else if (isSaca || b >= B_MLP) THROTTLE(WIN); else THROTTLE(WINM); } while (0)
    UNR for (int i = 0; i < 16; ++i) { K[i] = ldw<NT_KV>(kp + i * 4 * DH); THR_S; }
    SET_TAG;
    asm volatile("" ::"v"(pf));
    ST(1);
    UNR for (int i = 0; i < VQ; ++i) { V[i] = ldw<NT_KV>(vp + ((i >> 2) * 16 + (i & 3)) * DH); THR_S; }
    ST(13);
    wg_barrier();
    ST(4);
    UNR for (int i = VQ; i < VQ + VQ2; ++i) V[i] = ldw<NT_KV>(vp + ((i >> 2) * 16 + (i & 3)) * DH);
    fence();
    float sc[4];
    {
      const u32x4 aq = *(u32x4*)&L.q[16 * (n & 3) + 4 * g], z = {0u, 0u, 0u, 0u};
      u32x4 A[4];
      UNR for (int k = 0; k < 4; ++k) A[k] = (n >> 2) == k ? aq : z;
      UNR for (int G = 0; G < 4; ++G) {
        f32x4 acc = zero4;
        UNR for (int k = 0; k < 4; ++k) acc = mfma(A[k], K[4 * G + k], acc);
        const float v = (n & 2) ? ((n & 1) ? acc.w : acc.z) : ((n & 1) ? acc.y : acc.x);
        sc[G] = rbf(rbf(quad_sum(v)) * 0.08838834764831845f);
      }
    }
    const float mv = top_max(fmaxf(fmaxf(sc[0], sc[1]), fmaxf(sc[2], sc[3])));
    if (lane == 0) L.mw[sw] = mv;
    wg_barrier();
    const f32x4 ma = *(f32x4*)&L.mw[0], mc = *(f32x4*)&L.mw[4];
    const float m = fmaxf(fmaxf(fmaxf(ma.x, ma.y), fmaxf(ma.z, ma.w)), fmaxf(fmaxf(mc.x, mc.y), fmaxf(mc.z, mc.w)));
    u32 pw[4];
    float l = 0.f;
    UNR for (int G = 0; G < 4; ++G) { const float p = __expf(sc[G] - m), ph = rbf(p); l += p; pw[G] = pk2(ph, p - ph); }
    l = top_sum(l);
    if (lane == 0) L.lw[sw] = l;
    fence();
    UNR for (int i = VQ + VQ2; i < 16; ++i) { V[i] = ldw<NT_KV>(vp + ((i >> 2) * 16 + (i & 3)) * DH); if (i < 15) THR_S; }
    fence();
    ST(2);
    const u32x4 selP = mksel(n & 7, t);
    f32x4 acc = zero4;
    UNR for (int G = 0; G < 4; ++G) {
      acc = mfma(mkA(dppu<0x150>(pw[G]), selP), V[4 * G], acc);
      acc = mfma(mkA(dppu<0x154>(pw[G]), selP), V[4 * G + 1], acc);
      acc = mfma(mkA(dppu<0x158>(pw[G]), selP), V[4 * G + 2], acc);
      acc = mfma(mkA(dppu<0x15C>(pw[G]), selP), V[4 * G + 3], acc);
    }
    acc.x = sum32(acc.x); acc.y = sum32(acc.y); acc.z = sum32(acc.z); acc.w = sum32(acc.w);
    if (lane < 32) *(f32x4*)&L.ored[sw][8 * n + 4 * g] = acc;
    ST(14);
    wg_barrier();
    ST(5);
    if (isMlp) {
      if (sw < 5) {
        __builtin_amdgcn_s_sleep(4);
        const u16* src = P.wd + (size_t)(f0 + 8 * g) * D + 128 * sw + 8 * n;
        UNR for (int i = 0; i < 8; ++i) K[i] = ldw<NT_W>(src + i * D);
        u32x4 p[4], g0, g1;
        const int col = 128 * sw + 4 * (lane & 31);
        const u32x2 xx = *(const u32x2*)(P.x + col);
        SPIN(2, SPIN_CAP, g0 = ld4(ws, WS_MOD + col * 8); g1 = ld4(ws, WS_MOD + col * 8 + 16);
             const bool ok = (TAGBAD(g0.xy) | TAGBAD(g0.zw) | TAGBAD(g1.xy) | TAGBAD(g1.zw)) == 0)
        SPIN(1, SPIN_CAP, UNR for (int k = 0; k < 4; ++k) p[k] = ld4(ws, WS_WOP + ((2 * k + (lane >> 5)) * D + col) * 4); const bool ok = full16(p, 0))
        ST(7);
        const f32x4 a = (asf(p[0]) + asf(p[1])) + (asf(p[2]) + asf(p[3]));
        f32x4 x2;
        x2.x = rbf(lo(xx.x) + rbf(gval(g0.x) * rbf(sum32(a.x))));
        x2.y = rbf(hi(xx.x) + rbf(gval(g0.z) * rbf(sum32(a.y))));
        x2.z = rbf(lo(xx.y) + rbf(gval(g1.x) * rbf(sum32(a.z))));
        x2.w = rbf(hi(xx.y) + rbf(gval(g1.z) * rbf(sum32(a.w))));
        if (lane < 32) *(f32x4*)&L.x2[col] = x2;
        float sv = lane < 32 ? (x2.x + x2.y) + (x2.z + x2.w) : (x2.x * x2.x + x2.y * x2.y) + (x2.z * x2.z + x2.w * x2.w);
        sv = quad_sum(sv); sv += dpp<0x124>(sv); sv += dpp<0x128>(sv); sv = sum16(sv);
        if ((lane & 31) == 0) L.st[sw][lane >> 5] = sv;
      } else {
        u32x4 mg[5];
        SPIN(2, SPIN_CAP, u32 bad = 0;
             UNR for (int i = 0; i < 5; ++i) mg[i] = ld4(ws, WS_MOD + ((sw - 4) * D + 2 * lane + 128 * i) * 8);
             UNR for (int i = 0; i < 5; ++i) bad |= TAGBAD(mg[i].xy) | TAGBAD(mg[i].zw);
             const bool ok = bad == 0)
        UNR for (int i = 0; i < 5; ++i) { const f32x2 v = {gval(mg[i].x), gval(mg[i].z)}; *(f32x2*)&L.modv[sw - 5][2 * lane + 128 * i] = v; }
      }
      const u32x4 selL = mksel(n & 7, 0), selH = mksel(n & 7, 1);
      wg_barrier();
      wg_barrier();
      if (sw < 5) {
        ST(15);
        const float v = rbf(sum8(L, lane));
        auto r = __builtin_amdgcn_permlane32_swap(__float_as_uint(v), __float_as_uint(v), false, false);
        const float gv = __uint_as_float(r[0]), uv = __uint_as_float(r[1]);
        const float in2 = (2.f * 0.7978845608028654f) * (gv + 0.044715f * gv * gv * gv);
        const float th = 1.f - 2.f * __builtin_amdgcn_rcpf(__expf(in2) + 1.f);
        const u32 hb = pk2(rbf(0.5f * gv * (1.f + th)) * uv, 0.f);
        if (lane < 32) ((u16*)L.vec[wave])[lane] = (u16)hb;
        lds_wait();
        const u32x4 mm = *(u32x4*)&L.vec[wave][4 * g];
        f32x4 y = zero4;
        UNR for (int i = 0; i < 4; ++i) { y = mfma(mkA(mm[i], selL), K[2 * i], y); y = mfma(mkA(mm[i], selH), K[2 * i + 1], y); }
        if (g < 2) st4(ws, WS_DP + (fi * D + 128 * sw + 8 * n + 4 * g) * 4, canon4(y));
        ST(10);
      }
    }
  }
}

extern "C" void kernel_launch(void* const* d_in, const int* in_sizes, int n_in, void* d_out, int out_size,
                              void* d_ws, size_t ws_size, hipStream_t stream) {
  Params p;
  p.x = (const u16*)d_in[0]; p.kc = (const u16*)d_in[1]; p.vc = (const u16*)d_in[2];
  p.wq = (const u16*)d_in[3]; p.wo = (const u16*)d_in[4];
  p.wg = (const u16*)d_in[6]; p.wu = (const u16*)d_in[7]; p.wd = (const u16*)d_in[8];
  p.c = (const u16*)d_in[9]; p.wm = (const u16*)d_in[10];
  p.out = (u16*)d_out; p.ws = (char*)d_ws;
  adaln_block_kernel<<<256, 768, 0, stream>>>(p);
}
```

```cpp
#include <hip/hip_runtime.h>
#include <hip/hip_bf16.h>

typedef unsigned int u32;
typedef unsigned long long u64;
typedef unsigned short u16;
typedef __attribute__((ext_vector_type(4))) u32 u32x4;
typedef __attribute__((ext_vector_type(2))) u32 u32x2;
typedef __attribute__((ext_vector_type(4))) float f32x4;
typedef __attribute__((ext_vector_type(8))) __bf16 bf16x8;
typedef __attribute__((ext_vector_type(2))) __bf16 bf16x2;
typedef __attribute__((ext_vector_type(2))) float f32x2;
#define DEV __device__ __forceinline__

#define WIN 2
#define VQ2 2
#define WINM 3
#define WINH 4
#define VQ 8
#define NAP1 16
#define NT_G 1
#define WINC 4
#define PH1 6
#define NAP0 64
#define NT_KV 1
#define NT_W 1

constexpr int D = 640, DH = 128, SK = 4096, DF = 1728, NMOD = 3840;
constexpr int N_SACA = 80, B_MOD = 80, B_WO = 120, B_MLP = 200, N_MLP = 54, N_FIN = 40;
constexpr u32 WS_EPOCH = 0, WS_FLAG = 2048, WS_MOD = 4096, WS_H1 = 24576, WS_QP = 65536, WS_OP = 196608,
              WS_ML = 327680, WS_WOP = 344064, WS_DP = 393216, WS_BYTES = 655360, WS_STAMP = 1 << 20;
constexpr u32 EMPTY = 0xFFFFFFFFu;
constexpr u32 SPIN_CAP = 1u << 22;
constexpr int SC1 = 16;

struct Params {
  const u16 *x, *kc, *vc, *wq, *wo, *wg, *wu, *wd, *c, *wm;
  u16* out;
  char* ws;
};

DEV float bf2f(u32 v) { return __uint_as_float(v << 16); }
DEV u32 pk2(float lo, float hi) { f32x2 f = {lo, hi}; return __builtin_bit_cast(u32, __builtin_convertvector(f, bf16x2)); }
DEV u32 f2bf(float f) { return pk2(f, 0.f) & 0xFFFFu; }
DEV float rbf(float f) { return __uint_as_float(pk2(0.f, f) & 0xFFFF0000u); }
DEV u32 canon(float f) { return min(__float_as_uint(f), EMPTY - 1u); }
template <int C> DEV float dpp(float v) { return __int_as_float(__builtin_amdgcn_update_dpp(0, __float_as_int(v), C, 0xF, 0xF, true)); }
template <int C> DEV u32 dppu(u32 v) { return (u32)__builtin_amdgcn_update_dpp(0, (int)v, C, 0xF, 0xF, true); }
DEV float sum16(float v) { u32 u = __float_as_uint(v); auto r = __builtin_amdgcn_permlane16_swap(u, u, false, false); return __uint_as_float(r[0]) + __uint_as_float(r[1]); }
DEV float sum32(float v) { u32 u = __float_as_uint(v); auto r = __builtin_amdgcn_permlane32_swap(u, u, false, false); return __uint_as_float(r[0]) + __uint_as_float(r[1]); }
DEV float max16(float v) { u32 u = __float_as_uint(v); auto r = __builtin_amdgcn_permlane16_swap(u, u, false, false); return fmaxf(__uint_as_float(r[0]), __uint_as_float(r[1])); }
DEV float max32(float v) { u32 u = __float_as_uint(v); auto r = __builtin_amdgcn_permlane32_swap(u, u, false, false); return fmaxf(__uint_as_float(r[0]), __uint_as_float(r[1])); }
DEV float quad_sum(float v) { v += dpp<0xB1>(v); v += dpp<0x4E>(v); return v; }
DEV float top_sum(float v) { v += dpp<0x124>(v); v += dpp<0x128>(v); return sum32(sum16(v)); }
DEV float top_max(float v) { v = fmaxf(v, dpp<0x124>(v)); v = fmaxf(v, dpp<0x128>(v)); return max32(max16(v)); }
DEV float wave_sum(float v) { return top_sum(quad_sum(v)); }
DEV f32x4 mfma(u32x4 a, u32x4 b, f32x4 c) { return __builtin_amdgcn_mfma_f32_16x16x32_bf16(__builtin_bit_cast(bf16x8, a), __builtin_bit_cast(bf16x8, b), c, 0, 0, 0); }
DEV u32x4 mkA(u32 w, u32x4 s) { u32x4 a; a.x = __builtin_amdgcn_perm(w, w, s.x); a.y = __builtin_amdgcn_perm(w, w, s.y); a.z = __builtin_amdgcn_perm(w, w, s.z); a.w = __builtin_amdgcn_perm(w, w, s.w); return a; }
DEV u32x4 mksel(int j, int hi) { u32 nz = (j & 1) ? (hi ? 0x03020c0cu : 0x01000c0cu) : (hi ? 0x0c0c0302u : 0x0c0c0100u); int q = j >> 1; u32 z = 0x0c0c0c0cu;
  u32x4 s = {q == 0 ? nz : z, q == 1 ? nz : z, q == 2 ? nz : z, q == 3 ? nz : z}; return s; }
template <int NT> DEV u32x4 ldw(const u16* p) { if (NT) return __builtin_nontemporal_load((const u32x4*)p); else return *(const u32x4*)p; }
DEV void fence() { asm volatile("" ::: "memory"); }
DEV void wg_barrier() { asm volatile("s_waitcnt lgkmcnt(0)\n\ts_barrier" ::: "memory"); }
DEV void lds_wait() { asm volatile("s_waitcnt lgkmcnt(0)" ::: "memory"); }
typedef __amdgpu_buffer_rsrc_t rsrc_t;
DEV u32x4 ld4(rsrc_t r, u32 off) { return __builtin_amdgcn_raw_buffer_load_b128(r, off, 0, SC1); }
DEV u32x2 ld2(rsrc_t r, u32 off) { return __builtin_amdgcn_raw_buffer_load_b64(r, off, 0, SC1); }
DEV void st4(rsrc_t r, u32 off, u32x4 v) { __builtin_amdgcn_raw_buffer_store_b128(v, r, off, 0, SC1); }
DEV void st2(rsrc_t r, u32 off, u32x2 v) { __builtin_amdgcn_raw_buffer_store_b64(v, r, off, 0, SC1); }
DEV void st1(rsrc_t r, u32 off, u32 v) { __builtin_amdgcn_raw_buffer_store_b32(v, r, off, 0, SC1); }
DEV u32 mx3(u32 a, u32 b, u32 c) { return max(max(a, b), c); }
DEV u32 mx4(u32x4 v) { return max(mx3(v.x, v.y, v.z), v.w); }
DEV bool full16(const u32x4 (&p)[4], u32 x) { return mx3(mx3(mx3(p[0].x, p[0].y, p[0].z), mx3(p[0].w, p[1].x, p[1].y), mx3(p[1].z, p[1].w, p[2].x)), mx3(mx3(p[2].y, p[2].z, p[2].w), mx3(p[3].x, p[3].y, p[3].z), p[3].w), x) != EMPTY; }
DEV f32x4 asf(u32x4 v) { return __builtin_bit_cast(f32x4, v); }
DEV u32x4 canon4(f32x4 v) { u32x4 r = {canon(v.x), canon(v.y), canon(v.z), canon(v.w)}; return r; }
#define SPIN(nap, cap, cond) _Pragma("unroll 1") for (u32 spin_ = 0; spin_ < (cap); ++spin_) { fence(); cond; if (__all(ok)) break; __builtin_amdgcn_s_sleep(nap); }
#define TAGBAD(v) (((((v).x ^ tagLo) & 0xFFu) | ((v).y ^ tagHi)))
#define UNR _Pragma("unroll")
#define THROTTLE(w) asm volatile("s_waitcnt vmcnt(%0)" ::"n"(w) : "memory")
DEV int pairmap(int i, int n) { return i < (n & ~15) ? ((i & ~15) | ((i & 7) << 1) | ((i >> 3) & 1)) : i; }
DEV float silu_bf(float v) { return v * __builtin_amdgcn_rcpf(1.f + __expf(-v)); }
#define ST(k)

struct __attribute__((aligned(16))) Lds {
  float red[8][64];
  float ored[8][128];
  float x2[D];
  float modv[3][D];
  u32 q[64];
  u32 vec[12][80];
  float mw[8], lw[8], st[8][2];
  u32 cnt[4];
};

DEV void cbar(Lds& L, u32& target, int lane) {
  target += 4;
  lds_wait();
  if (lane == 0) __hip_atomic_fetch_add(&L.cnt[0], 1u, __ATOMIC_RELAXED, __HIP_MEMORY_SCOPE_WORKGROUP);
#pragma unroll 1
  for (u32 s = 0; s < SPIN_CAP; ++s) { fence(); if (__hip_atomic_load(&L.cnt[0], __ATOMIC_RELAXED, __HIP_MEMORY_SCOPE_WORKGROUP) >= target) break; }
  fence();
}
DEV void ln_fin(float s1, float s2, float& mean_b, float& rsd) {
  const float m = s1 * (1.f / D), var = rbf(fmaxf(s2 * (1.f / D) - m * m, 0.f));
  mean_b = rbf(m);
  rsd = __builtin_amdgcn_rcpf(rbf(__builtin_amdgcn_sqrtf(rbf(var + 1e-5f))));
}
DEV float ln_mod(float x, float mean_b, float rsd, float sc, float sh) { return rbf(rbf(rbf(x - mean_b) * rsd) * rbf(1.f + sc)) + sh; }
DEV float lo(u32 v) { return __uint_as_float(v << 16); }
DEV float hi(u32 v) { return __uint_as_float(v & 0xFFFF0000u); }
DEV float gval(u32 v) { return __uint_as_float(v & 0xFFFFFF00u); }
DEV float sum8(const Lds& L, int lane) { return ((L.red[0][lane] + L.red[1][lane]) + (L.red[2][lane] + L.red[3][lane])) + ((L.red[4][lane] + L.red[5][lane]) + (L.red[6][lane] + L.red[7][lane])); }

__global__ __launch_bounds__(768, 3) void adaln_block_kernel(Params P) {
  __shared__ Lds L;
  const int tid = threadIdx.x, lane = tid & 63, b = blockIdx.x;
  const int wave = __builtin_amdgcn_readfirstlane(tid >> 6);
  const int g = lane >> 4, n = lane & 15, t = n >> 3;
  const int h = b >> 3, s = b & 7;
  const bool isSaca = b < N_SACA, isMod = b >= B_MOD && b < B_WO, isWo = b >= B_WO && b < B_MLP, isMlp = b >= B_MLP && b < B_MLP + N_MLP;
  const rsrc_t ws = __builtin_amdgcn_make_buffer_rsrc(P.ws, 0, WS_BYTES, 0x00020000);
  const f32x4 zero4 = {0.f, 0.f, 0.f, 0.f};
  const int fi = b - B_MLP, f0 = 32 * pairmap(fi, N_MLP);
  ST(0);
  const u32x2 ep = ld2(ws, WS_EPOCH);
#define SET_TAG const u64 e1 = (((u64)(u32)__builtin_amdgcn_readfirstlane(ep.y) << 32) | (u32)__builtin_amdgcn_readfirstlane(ep.x)) + 1; const u32 tagLo = (u32)e1 & 0xFFu, tagHi = (u32)(e1 >> 8)
  if (wave < 4) {
    const int cw = wave, r8 = 2 * g + t, r20 = 160 * cw + 20 * r8;
    const int wp = (b - B_WO) / 10, wc = (b - B_WO) % 10;
    if (tid == 0) L.cnt[0] = 0;
    u32 nb = 0;
    u32x4 W[20], WQ[5];
    u32 cv[10], xv[5];
    const int line = b % 20, kq = b / 20, rs = 160 * kq + 40 * cw + 2 * r8;
    if (isSaca) {
      UNR for (int i = 0; i < 5; ++i) W[i] = ldw<NT_W>(P.wm + (size_t)(i < 4 ? rs + 16 * (i >> 1) + (i & 1) : rs + 32 - r8) * NMOD + 64 * line + 8 * (n & 7));
      cv[0] = ((const u32*)P.c)[rs >> 1]; cv[1] = ((const u32*)P.c)[(rs >> 1) + 8]; cv[2] = ((const u32*)P.c)[(rs + 32 - r8) >> 1];
    }
    const int rq = 80 * s + 20 * cw + 5 * g, mr = rq + n % 5;
    UNR for (int i = 0; i < 5; ++i) xv[i] = ((const u32*)P.x)[lane + 64 * i];
    const u32 xs = ((const u32*)P.x)[mr >> 1];
    UNR for (int i = 0; i < 5; ++i) WQ[i] = ldw<NT_W>(P.wq + (size_t)(rq + i) * 4096 + h * DH + 8 * n);
    if (cw == 3) {
      const u32x4 e = {EMPTY, EMPTY, EMPTY, EMPTY};
      st4(ws, (lane < 32 ? WS_QP : WS_OP - 512) + b * 512 + lane * 16, e);
      if (lane == 0) st4(ws, WS_ML + b * 64, e);
      if (isWo && lane < 16) st4(ws, WS_WOP + (wp * D + wc * 64) * 4 + lane * 16, e);
      if (isMlp) for (int i = lane; i < 160; i += 64) st4(ws, WS_DP + fi * (D * 4) + i * 16, e);
    }
    wg_barrier();
    SET_TAG;
    const u32x4 selL = mksel(n & 7, 0), selH = mksel(n & 7, 1);
    if (isSaca) {
      const u32 w0 = pk2(silu_bf(lo(cv[0])), silu_bf(hi(cv[0]))), w1 = pk2(silu_bf(lo(cv[1])), silu_bf(hi(cv[1])));
      const u32 w2 = pk2(silu_bf(t ? hi(cv[2]) : lo(cv[2])), 0.f);
      f32x4 acc = mfma(mkA(w0, selL), W[0], zero4);
      acc = mfma(mkA(w0, selH), W[1], acc);
      acc = mfma(mkA(w1, selL), W[2], acc);
      acc = mfma(mkA(w1, selH), W[3], acc);
      acc = mfma(mkA(w2, selL), W[4], acc);
      if ((g >> 1) == t) *(f32x4*)&L.red[2 * cw + t][8 * (n & 7) + 4 * (g & 1)] = acc;
      cbar(L, nb, lane);
      if (cw == 0) { const u32x2 gr = {((__float_as_uint(sum8(L, lane)) + 0x80u) & 0xFFFFFF00u) | tagLo, tagHi}; st2(ws, WS_H1 + (64 * line + lane) * 32 + kq * 8, gr); }
    }
    if (cw == 3) {
      asm volatile("s_waitcnt vmcnt(0)" ::: "memory");
      if (lane == 0) { const u32x2 f = {tagLo, tagHi}; st2(ws, WS_FLAG + b * 8, f); }
    }
    float mb, rsd;
    {
      float s1 = 0.f, s2 = 0.f;
      UNR for (int i = 0; i < 5; ++i) { const float a = lo(xv[i]), c = hi(xv[i]); s1 += a + c; s2 += a * a + c * c; }
      ln_fin(wave_sum(s1), wave_sum(s2), mb, rsd);
    }
    const bool role = isWo || isMlp || isMod;
    const u16* wsrc = isWo ? P.wo + (size_t)(128 * (4 * wp + cw) + 16 * r8) * D + 64 * wc + 8 * (n & 7)
                    : isMlp ? (((n >> 2) & 1) ? P.wu : P.wg) + (size_t)r20 * DF + f0 + 8 * (n & 3)
                            : P.wm + (size_t)r20 * NMOD + 1280 + 64 * (b - B_MOD) + 8 * (n & 7);
    const int wstr = isWo ? D : isMlp ? DF : NMOD;
    ST(1);
    if (role) {
      if (isMod) { UNR for (int i = 0; i < 5; ++i) { const u32x2 v = *(const u32x2*)(P.c + r20 + 4 * i); cv[2 * i] = v.x; cv[2 * i + 1] = v.y; } }
      UNR for (int i = 0; i < PH1; ++i) { W[i] = ldw<NT_W>(wsrc + i * wstr); THROTTLE(WINC); }
    } else if (!isSaca) __builtin_amdgcn_s_sleep(NAP0);
    {
      u32x4 p0, p1;
      const u32 off = WS_H1 + (mr + (n >= 5 && n < 10 ? D : 0)) * 32;
      SPIN(NAP1, SPIN_CAP, const u32x2 f = ld2(ws, cw == 3 ? WS_FLAG + (8 * h + (lane & 7)) * 8 : off); p0 = ld4(ws, off); p1 = ld4(ws, off + 16);
           const bool ok = (TAGBAD(f) | TAGBAD(p0.xy) | TAGBAD(p0.zw) | TAGBAD(p1.xy) | TAGBAD(p1.zw)) == 0)
      ST(2);
      const float v = rbf((gval(p0.x) + gval(p0.z)) + (gval(p1.x) + gval(p1.z)));
      const u32 hb = pk2(ln_mod((mr & 1) ? hi(xs) : lo(xs), mb, rsd, dpp<0x105>(v), v), 0.f);
      f32x4 acc = mfma(mkA(dppu<0x150>(hb), selL), WQ[0], zero4);
      acc = mfma(mkA(dppu<0x151>(hb), selL), WQ[1], acc);
      acc = mfma(mkA(dppu<0x152>(hb), selL), WQ[2], acc);
      acc = mfma(mkA(dppu<0x153>(hb), selL), WQ[3], acc);
      acc = mfma(mkA(dppu<0x154>(hb), selL), WQ[4], acc);
      if (g < 2) *(f32x4*)&L.ored[cw][8 * n + 4 * g] = acc;
      cbar(L, nb, lane);
    }
    if (cw == 0) {
      const int l4 = 4 * (lane & 31);
      const f32x4 v0 = (*(f32x4*)&L.ored[0][l4] + *(f32x4*)&L.ored[1][l4]) + (*(f32x4*)&L.ored[2][l4] + *(f32x4*)&L.ored[3][l4]);
      if (lane < 32) st4(ws, WS_QP + b * 512 + lane * 16, canon4(v0));
      u32x4 p[4];
      SPIN(2, SPIN_CAP, UNR for (int k = 0; k < 4; ++k) p[k] = ld4(ws, WS_QP + h * 4096 + k * 1024 + lane * 16); const bool ok = full16(p, 0))
      const f32x4 v = (asf(p[0]) + asf(p[1])) + (asf(p[2]) + asf(p[3]));
      const u32x2 qq = {pk2(sum32(v.x), sum32(v.y)), pk2(sum32(v.z), sum32(v.w))};
      if (lane < 32) *(u32x2*)&L.q[2 * lane] = qq;
    }
    ST(3);
    if (cw == 3 && (isWo || isMlp)) {
      SPIN(NAP1, SPIN_CAP, const u32x4 f0v = ld4(ws, WS_FLAG + lane * 32); const u32x4 f1v = ld4(ws, WS_FLAG + lane * 32 + 16);
           const bool ok = (TAGBAD(f0v.xy) | TAGBAD(f0v.zw) | TAGBAD(f1v.xy) | TAGBAD(f1v.zw)) == 0)
    }
    if (cw == 0) { wg_barrier(); ST(4); wg_barrier(); }
    if (role) { UNR for (int i = PH1; i < 20; ++i) if (i < 16 || !isWo) { W[i] = ldw<NT_W>(wsrc + i * wstr); THROTTLE(WINC); } }
    ST(13);
    if (cw != 0) { wg_barrier(); ST(4); wg_barrier(); }
    if (isMod) {
      f32x4 acc = zero4;
      UNR for (int i = 0; i < 10; ++i) {
        const u32 w = pk2(silu_bf(lo(cv[i])), silu_bf(hi(cv[i])));
        acc = mfma(mkA(w, selL), W[2 * i], acc);
        acc = mfma(mkA(w, selH), W[2 * i + 1], acc);
      }
      if ((g >> 1) == t) *(f32x4*)&L.red[2 * cw + t][8 * (n & 7) + 4 * (g & 1)] = acc;
      cbar(L, nb, lane);
      if (cw == 1) { const u32x2 gr = {__float_as_uint(rbf(sum8(L, lane))) | tagLo, tagHi}; st2(ws, WS_MOD + (64 * (b - B_MOD) + lane) * 8, gr); }
    }
    const float mwg = fmaxf(fmaxf(fmaxf(L.mw[0], L.mw[1]), fmaxf(L.mw[2], L.mw[3])), fmaxf(fmaxf(L.mw[4], L.mw[5]), fmaxf(L.mw[6], L.mw[7])));
    wg_barrier();
    ST(5);
    if (cw == 0) {
      const int l4 = 4 * (lane & 31), k0 = 4 * (lane >> 5);
      f32x4 o = (*(f32x4*)&L.ored[k0][l4] + *(f32x4*)&L.ored[k0 + 1][l4]) + (*(f32x4*)&L.ored[k0 + 2][l4] + *(f32x4*)&L.ored[k0 + 3][l4]);
      const f32x4 la = *(f32x4*)&L.lw[0] + *(f32x4*)&L.lw[4];
      o.x = sum32(o.x); o.y = sum32(o.y); o.z = sum32(o.z); o.w = sum32(o.w);
      const u32x2 mlv = {canon(mwg), canon((la.x + la.y) + (la.z + la.w))};
      if (lane < 32) st4(ws, WS_OP + b * 512 + lane * 16, canon4(o));
      if (lane == 32) st2(ws, WS_ML + b * 64, mlv);
      ST(6);
    }
    if (isWo) {
      const int hh = 4 * wp + cw;
      u32x4 p[4]; u32x2 mlv;
      SPIN(1, SPIN_CAP, mlv = ld2(ws, WS_ML + (hh * 8 + (lane & 7)) * 64);
           UNR for (int k = 0; k < 4; ++k) p[k] = ld4(ws, WS_OP + hh * 4096 + k * 1024 + lane * 16);
           const bool ok = full16(p, max(mlv.x, mlv.y)))
      ST(7);
      const float mw = __uint_as_float(mlv.x), lw = __uint_as_float(mlv.y);
      float m = fmaxf(mw, dpp<0xB1>(mw)); m = fmaxf(m, dpp<0x4E>(m)); m = fmaxf(m, dpp<0x141>(m));
      float e = __expf(mw - m);
      float l = quad_sum(e * lw); l += dpp<0x141>(l);
      e *= __builtin_amdgcn_rcpf(l);
      e = lane < 32 ? e : dpp<0xB1>(e);
      const f32x4 o = (dpp<0x150>(e) * asf(p[0]) + dpp<0x152>(e) * asf(p[1])) + (dpp<0x154>(e) * asf(p[2]) + dpp<0x156>(e) * asf(p[3]));
      const u32x2 ob = {pk2(sum32(o.x), sum32(o.y)), pk2(sum32(o.z), sum32(o.w))};
      if (lane < 32) *(u32x2*)&L.vec[wave][2 * lane] = ob;
      lds_wait();
      const u32x4 m0 = *(u32x4*)&L.vec[wave][8 * r8], m1 = *(u32x4*)&L.vec[wave][8 * r8 + 4];
      f32x4 acc = zero4;
      UNR for (int i = 0; i < 8; ++i) {
        const u32 w = i < 4 ? m0[i & 3] : m1[i & 3];
        acc = mfma(mkA(w, selL), W[2 * i], acc);
        acc = mfma(mkA(w, selH), W[2 * i + 1], acc);
      }
      if ((g >> 1) == t) *(f32x4*)&L.red[2 * cw + t][8 * (n & 7) + 4 * (g & 1)] = acc;
      cbar(L, nb, lane);
      if (cw == 0) st1(ws, WS_WOP + (wp * D + 64 * wc + lane) * 4, canon(sum8(L, lane)));
      ST(8);
    }
    if (isMlp) {
      wg_barrier();
      ST(9);
      const int r = 160 * cw + 4 * (lane < 40 ? lane : 0);
      const f32x4 s01 = *(f32x4*)&L.st[0][0], s23 = *(f32x4*)&L.st[2][0], x2 = *(f32x4*)&L.x2[r], sb = *(f32x4*)&L.modv[0][r], cb = *(f32x4*)&L.modv[1][r];
      ln_fin((s01.x + s01.z) + (s23.x + s23.z) + L.st[4][0], (s01.y + s01.w) + (s23.y + s23.w) + L.st[4][1], mb, rsd);
      const u32x2 hb = {pk2(ln_mod(x2.x, mb, rsd, cb.x, sb.x), ln_mod(x2.y, mb, rsd, cb.y, sb.y)), pk2(ln_mod(x2.z, mb, rsd, cb.z, sb.z), ln_mod(x2.w, mb, rsd, cb.w, sb.w))};
      if (lane < 40) *(u32x2*)&L.vec[wave][2 * lane] = hb;
      lds_wait();
      f32x4 acc = zero4;
      UNR for (int i = 0; i < 5; ++i) {
        const u32x2 w = *(u32x2*)&L.vec[wave][10 * r8 + 2 * i];
        acc = mfma(mkA(w.x, selL), W[4 * i], acc);
        acc = mfma(mkA(w.x, selH), W[4 * i + 1], acc);
        acc = mfma(mkA(w.y, selL), W[4 * i + 2], acc);
        acc = mfma(mkA(w.y, selH), W[4 * i + 3], acc);
      }
      if ((g >> 1) == t) *(f32x4*)&L.red[2 * cw + t][8 * (n & 7) + 4 * (g & 1)] = acc;
      wg_barrier();
      if (cw == 0 && fi < N_FIN) {
        u32x4 p[4];
        const int pp = lane >> 2, col = 16 * fi + 4 * (lane & 3);
        const bool has3 = pp + 48 < N_MLP;
        const f32x4 xo = *(f32x4*)&L.x2[col], gb = *(f32x4*)&L.modv[2][col];
        SPIN(1, SPIN_CAP, UNR for (int k = 0; k < 4; ++k) p[k] = ld4(ws, WS_DP + col * 4 + (k < 3 || has3 ? pp + 16 * k : pp) * (D * 4)); const bool ok = full16(p, 0))
        ST(11);
        const f32x4 y = (asf(p[0]) + asf(p[1])) + (asf(p[2]) + (has3 ? asf(p[3]) : zero4));
        const u32x2 o = {pk2(xo.x + rbf(gb.x * rbf(top_sum(y.x))), xo.y + rbf(gb.y * rbf(top_sum(y.y)))),
                         pk2(xo.z + rbf(gb.z * rbf(top_sum(y.z))), xo.w + rbf(gb.w * rbf(top_sum(y.w))))};
        if (lane < 4) __builtin_amdgcn_raw_buffer_store_b64(o, __builtin_amdgcn_make_buffer_rsrc(P.out, 0, 2 * D, 0x00020000), col * 2, 0, 17);
        if (fi == 0 && lane == 0) { const u32x2 e = {(u32)e1, (u32)(e1 >> 32)}; st2(ws, WS_EPOCH, e); }
        ST(12);
      }
    }
  } else {
    const int sw = wave - 4;
    const size_t kb = ((size_t)h * SK + 512 * s + 64 * sw) * DH;
    const u16* kp = P.kc + kb + (n >> 2) * DH + 32 * (n & 3) + 8 * g;
    const u16* vp = P.vc + kb + 4 * g * DH + 8 * n;
    u32x4 K[16], V[16];
    u32 pf = 0;
    if (isMlp && sw == 7) pf = *(const u32*)(lane < 32 ? P.wd + (size_t)(f0 + lane) * D : P.out + 2 * (lane & 31));
    wg_barrier();
    #define THR_S do { if (isMlp) THROTTLE(WINH); else if (isSaca || b >= B_MLP) THROTTLE(WIN); else THROTTLE(WINM); } while (0)
    UNR for (int i = 0; i < 16; ++i) { K[i] = ldw<NT_KV>(kp + i * 4 * DH); THR_S; }
    SET_TAG;
    asm volatile("" ::"v"(pf));
    ST(1);
    UNR for (int i = 0; i < VQ; ++i) { V[i] = ldw<NT_KV>(vp + ((i >> 2) * 16 + (i & 3)) * DH); THR_S; }
    ST(13);
    wg_barrier();
    ST(4);
    UNR for (int i = VQ; i < VQ + VQ2; ++i) V[i] = ldw<NT_KV>(vp + ((i >> 2) * 16 + (i & 3)) * DH);
    fence();
    float sc[4];
    {
      const u32x4 aq = *(u32x4*)&L.q[16 * (n & 3) + 4 * g], z = {0u, 0u, 0u, 0u};
      u32x4 A[4];
      UNR for (int k = 0; k < 4; ++k) A[k] = (n >> 2) == k ? aq : z;
      UNR for (int G = 0; G < 4; ++G) {
        f32x4 acc = zero4;
        UNR for (int k = 0; k < 4; ++k) acc = mfma(A[k], K[4 * G + k], acc);
        const float v = (n & 2) ? ((n & 1) ? acc.w : acc.z) : ((n & 1) ? acc.y : acc.x);
        sc[G] = rbf(rbf(quad_sum(v)) * 0.08838834764831845f);
      }
    }
    const float mv = top_max(fmaxf(fmaxf(sc[0], sc[1]), fmaxf(sc[2], sc[3])));
    if (lane == 0) L.mw[sw] = mv;
    wg_barrier();
    const f32x4 ma = *(f32x4*)&L.mw[0], mc = *(f32x4*)&L.mw[4];
    const float m = fmaxf(fmaxf(fmaxf(ma.x, ma.y), fmaxf(ma.z, ma.w)), fmaxf(fmaxf(mc.x, mc.y), fmaxf(mc.z, mc.w)));
    u32 pw[4];
    float l = 0.f;
    UNR for (int G = 0; G < 4; ++G) { const float p = __expf(sc[G] - m), ph = rbf(p); l += p; pw[G] = pk2(ph, p - ph); }
    l = top_sum(l);
    if (lane == 0) L.lw[sw] = l;
    fence();
    UNR for (int i = VQ + VQ2; i < 16; ++i) { V[i] = ldw<NT_KV>(vp + ((i >> 2) * 16 + (i & 3)) * DH); if (i < 15) THR_S; }
    fence();
    ST(2);
    const u32x4 selP = mksel(n & 7, t);
    f32x4 acc = zero4;
    UNR for (int G = 0; G < 4; ++G) {
      acc = mfma(mkA(dppu<0x150>(pw[G]), selP), V[4 * G], acc);
      acc = mfma(mkA(dppu<0x154>(pw[G]), selP), V[4 * G + 1], acc);
      acc = mfma(mkA(dppu<0x158>(pw[G]), selP), V[4 * G + 2], acc);
      acc = mfma(mkA(dppu<0x15C>(pw[G]), selP), V[4 * G + 3], acc);
    }
    acc.x = sum32(acc.x); acc.y = sum32(acc.y); acc.z = sum32(acc.z); acc.w = sum32(acc.w);
    if (lane < 32) *(f32x4*)&L.ored[sw][8 * n + 4 * g] = acc;
    ST(14);
    wg_barrier();
    ST(5);
    if (isMlp) {
      if (sw < 5) {
        __builtin_amdgcn_s_sleep(4);
        const u16* src = P.wd + (size_t)(f0 + 8 * g) * D + 128 * sw + 8 * n;
        UNR for (int i = 0; i < 8; ++i) K[i] = ldw<NT_W>(src + i * D);
        u32x4 p[4], g0, g1;
        const int col = 128 * sw + 4 * (lane & 31);
        const u32x2 xx = *(const u32x2*)(P.x + col);
        SPIN(2, SPIN_CAP, g0 = ld4(ws, WS_MOD + col * 8); g1 = ld4(ws, WS_MOD + col * 8 + 16);
             const bool ok = (TAGBAD(g0.xy) | TAGBAD(g0.zw) | TAGBAD(g1.xy) | TAGBAD(g1.zw)) == 0)
        SPIN(1, SPIN_CAP, UNR for (int k = 0; k < 4; ++k) p[k] = ld4(ws, WS_WOP + ((2 * k + (lane >> 5)) * D + col) * 4); const bool ok = full16(p, 0))
        ST(7);
        const f32x4 a = (asf(p[0]) + asf(p[1])) + (asf(p[2]) + asf(p[3]));
        f32x4 x2;
        x2.x = rbf(lo(xx.x) + rbf(gval(g0.x) * rbf(sum32(a.x))));
        x2.y = rbf(hi(xx.x) + rbf(gval(g0.z) * rbf(sum32(a.y))));
        x2.z = rbf(lo(xx.y) + rbf(gval(g1.x) * rbf(sum32(a.z))));
        x2.w = rbf(hi(xx.y) + rbf(gval(g1.z) * rbf(sum32(a.w))));
        if (lane < 32) *(f32x4*)&L.x2[col] = x2;
        float sv = lane < 32 ? (x2.x + x2.y) + (x2.z + x2.w) : (x2.x * x2.x + x2.y * x2.y) + (x2.z * x2.z + x2.w * x2.w);
        sv = quad_sum(sv); sv += dpp<0x124>(sv); sv += dpp<0x128>(sv); sv = sum16(sv);
        if ((lane & 31) == 0) L.st[sw][lane >> 5] = sv;
      } else {
        u32x4 mg[5];
        SPIN(2, SPIN_CAP, u32 bad = 0;
             UNR for (int i = 0; i < 5; ++i) mg[i] = ld4(ws, WS_MOD + ((sw - 4) * D + 2 * lane + 128 * i) * 8);
             UNR for (int i = 0; i < 5; ++i) bad |= TAGBAD(mg[i].xy) | TAGBAD(mg[i].zw);
             const bool ok = bad == 0)
        UNR for (int i = 0; i < 5; ++i) { const f32x2 v = {gval(mg[i].x), gval(mg[i].z)}; *(f32x2*)&L.modv[sw - 5][2 * lane + 128 * i] = v; }
      }
      const u32x4 selL = mksel(n & 7, 0), selH = mksel(n & 7, 1);
      wg_barrier();
      wg_barrier();
      if (sw < 5) {
        ST(15);
        const float v = rbf(sum8(L, lane));
        auto r = __builtin_amdgcn_permlane32_swap(__float_as_uint(v), __float_as_uint(v), false, false);
        const float gv = __uint_as_float(r[0]), uv = __uint_as_float(r[1]);
        const float in2 = (2.f * 0.7978845608028654f) * (gv + 0.044715f * gv * gv * gv);
        const float th = 1.f - 2.f * __builtin_amdgcn_rcpf(__expf(in2) + 1.f);
        const u32 hb = pk2(rbf(0.5f * gv * (1.f + th)) * uv, 0.f);
        if (lane < 32) ((u16*)L.vec[wave])[lane] = (u16)hb;
        lds_wait();
        const u32x4 mm = *(u32x4*)&L.vec[wave][4 * g];
        f32x4 y = zero4;
        UNR for (int i = 0; i < 4; ++i) { y = mfma(mkA(mm[i], selL), K[2 * i], y); y = mfma(mkA(mm[i], selH), K[2 * i + 1], y); }
        if (g < 2) st4(ws, WS_DP + (fi * D + 128 * sw + 8 * n + 4 * g) * 4, canon4(y));
        ST(10);
      }
    }
  }
}

extern "C" void kernel_launch(void* const* d_in, const int* in_sizes, int n_in, void* d_out, int out_size,
                              void* d_ws, size_t ws_size, hipStream_t stream) {
  Params p;
  p.x = (const u16*)d_in[0]; p.kc = (const u16*)d_in[1]; p.vc = (const u16*)d_in[2];
  p.wq = (const u16*)d_in[3]; p.wo = (const u16*)d_in[4];
  p.wg = (const u16*)d_in[6]; p.wu = (const u16*)d_in[7]; p.wd = (const u16*)d_in[8];
  p.c = (const u16*)d_in[9]; p.wm = (const u16*)d_in[10];
  p.out = (u16*)d_out; p.ws = (char*)d_ws;
  adaln_block_kernel<<<256, 768, 0, stream>>>(p);
}
```

```cpp
#include <hip/hip_runtime.h>
#include <hip/hip_bf16.h>

typedef unsigned int u32;
typedef unsigned long long u64;
typedef unsigned short u16;
typedef __attribute__((ext_vector_type(4))) u32 u32x4;
typedef __attribute__((ext_vector_type(2))) u32 u32x2;
typedef __attribute__((ext_vector_type(4))) float f32x4;
typedef __attribute__((ext_vector_type(8))) __bf16 bf16x8;
typedef __attribute__((ext_vector_type(2))) __bf16 bf16x2;
typedef __attribute__((ext_vector_type(2))) float f32x2;
#define DEV __device__ __forceinline__

#define WIN 2
#define VQ2 2
#define EARLY0 0
#define PH2M 12
#define WINC2 6
#define DLATE 0
#define NAPA 1
#define NAPB 1
#define NAPC 1
#define WINM 4
#define WINH 5
#define VQ 10
#define NAP1 16
#define WINC 4
#define PH1 6
#define NAP0 64
#define NT_KV 1
#define NT_W 1

constexpr int D = 640, DH = 128, SK = 4096, DF = 1728, NMOD = 3840;
constexpr int N_SACA = 80, B_MOD = 80, B_WO = 120, B_MLP = 200, N_MLP = 54, N_FIN = 40;
constexpr u32 WS_EPOCH = 0, WS_FLAG = 2048, WS_MOD = 4096, WS_H1 = 24576, WS_QP = 65536, WS_OP = 196608,
              WS_ML = 327680, WS_WOP = 344064, WS_DP = 393216, WS_BYTES = 655360;
constexpr u32 EMPTY = 0xFFFFFFFFu;
constexpr u32 SPIN_CAP = 1u << 22;
constexpr int SC1 = 16;

struct Params {
  const u16 *x, *kc, *vc, *wq, *wo, *wg, *wu, *wd, *c, *wm;
  u16* out;
  char* ws;
};

DEV float bf2f(u32 v) { return __uint_as_float(v << 16); }
DEV u32 pk2(float lo, float hi) { f32x2 f = {lo, hi}; return __builtin_bit_cast(u32, __builtin_convertvector(f, bf16x2)); }
DEV u32 f2bf(float f) { return pk2(f, 0.f) & 0xFFFFu; }
DEV float rbf(float f) { return __uint_as_float(pk2(0.f, f) & 0xFFFF0000u); }
DEV u32 canon(float f) { return min(__float_as_uint(f), EMPTY - 1u); }
template <int C> DEV float dpp(float v) { return __int_as_float(__builtin_amdgcn_update_dpp(0, __float_as_int(v), C, 0xF, 0xF, true)); }
template <int C> DEV u32 dppu(u32 v) { return (u32)__builtin_amdgcn_update_dpp(0, (int)v, C, 0xF, 0xF, true); }
DEV float sum16(float v) { u32 u = __float_as_uint(v); auto r = __builtin_amdgcn_permlane16_swap(u, u, false, false); return __uint_as_float(r[0]) + __uint_as_float(r[1]); }
DEV float sum32(float v) { u32 u = __float_as_uint(v); auto r = __builtin_amdgcn_permlane32_swap(u, u, false, false); return __uint_as_float(r[0]) + __uint_as_float(r[1]); }
DEV float max16(float v) { u32 u = __float_as_uint(v); auto r = __builtin_amdgcn_permlane16_swap(u, u, false, false); return fmaxf(__uint_as_float(r[0]), __uint_as_float(r[1])); }
DEV float max32(float v) { u32 u = __float_as_uint(v); auto r = __builtin_amdgcn_permlane32_swap(u, u, false, false); return fmaxf(__uint_as_float(r[0]), __uint_as_float(r[1])); }
DEV float quad_sum(float v) { v += dpp<0xB1>(v); v += dpp<0x4E>(v); return v; }
DEV float top_sum(float v) { v += dpp<0x124>(v); v += dpp<0x128>(v); return sum32(sum16(v)); }
DEV float top_max(float v) { v = fmaxf(v, dpp<0x124>(v)); v = fmaxf(v, dpp<0x128>(v)); return max32(max16(v)); }
DEV float wave_sum(float v) { return top_sum(quad_sum(v)); }
DEV f32x4 mfma(u32x4 a, u32x4 b, f32x4 c) { return __builtin_amdgcn_mfma_f32_16x16x32_bf16(__builtin_bit_cast(bf16x8, a), __builtin_bit_cast(bf16x8, b), c, 0, 0, 0); }
DEV u32x4 mkA(u32 w, u32x4 s) { u32x4 a; a.x = __builtin_amdgcn_perm(w, w, s.x); a.y = __builtin_amdgcn_perm(w, w, s.y); a.z = __builtin_amdgcn_perm(w, w, s.z); a.w = __builtin_amdgcn_perm(w, w, s.w); return a; }
DEV u32x4 mksel(int j, int hi) { u32 nz = (j & 1) ? (hi ? 0x03020c0cu : 0x01000c0cu) : (hi ? 0x0c0c0302u : 0x0c0c0100u); int q = j >> 1; u32 z = 0x0c0c0c0cu;
  u32x4 s = {q == 0 ? nz : z, q == 1 ? nz : z, q == 2 ? nz : z, q == 3 ? nz : z}; return s; }
template <int NT> DEV u32x4 ldw(const u16* p) { if (NT) return __builtin_nontemporal_load((const u32x4*)p); else return *(const u32x4*)p; }
DEV void fence() { asm volatile("" ::: "memory"); }
DEV void wg_barrier() { asm volatile("s_waitcnt lgkmcnt(0)\n\ts_barrier" ::: "memory"); }
DEV void lds_wait() { asm volatile("s_waitcnt lgkmcnt(0)" ::: "memory"); }
typedef __amdgpu_buffer_rsrc_t rsrc_t;
DEV u32x4 ld4(rsrc_t r, u32 off) { return __builtin_amdgcn_raw_buffer_load_b128(r, off, 0, SC1); }
DEV u32x2 ld2(rsrc_t r, u32 off) { return __builtin_amdgcn_raw_buffer_load_b64(r, off, 0, SC1); }
DEV void st4(rsrc_t r, u32 off, u32x4 v) { __builtin_amdgcn_raw_buffer_store_b128(v, r, off, 0, SC1); }
DEV void st2(rsrc_t r, u32 off, u32x2 v) { __builtin_amdgcn_raw_buffer_store_b64(v, r, off, 0, SC1); }
DEV void st1(rsrc_t r, u32 off, u32 v) { __builtin_amdgcn_raw_buffer_store_b32(v, r, off, 0, SC1); }
DEV u32 mx3(u32 a, u32 b, u32 c) { return max(max(a, b), c); }
DEV u32 mx4(u32x4 v) { return max(mx3(v.x, v.y, v.z), v.w); }
DEV bool full16(const u32x4 (&p)[4], u32 x) { return mx3(mx3(mx3(p[0].x, p[0].y, p[0].z), mx3(p[0].w, p[1].x, p[1].y), mx3(p[1].z, p[1].w, p[2].x)), mx3(mx3(p[2].y, p[2].z, p[2].w), mx3(p[3].x, p[3].y, p[3].z), p[3].w), x) != EMPTY; }
DEV f32x4 asf(u32x4 v) { return __builtin_bit_cast(f32x4, v); }
DEV u32x4 canon4(f32x4 v) { u32x4 r = {canon(v.x), canon(v.y), canon(v.z), canon(v.w)}; return r; }
#define SPIN(nap, cap, cond) _Pragma("unroll 1") for (u32 spin_ = 0; spin_ < (cap); ++spin_) { fence(); cond; if (__all(ok)) break; __builtin_amdgcn_s_sleep(nap); }
#define TAGBAD(v) (((((v).x ^ tagLo) & 0xFFu) | ((v).y ^ tagHi)))
#define UNR _Pragma("unroll")
#define THROTTLE(w) asm volatile("s_waitcnt vmcnt(%0)" ::"n"(w) : "memory")
DEV int pairmap(int i, int n) { return i < (n & ~15) ? ((i & ~15) | ((i & 7) << 1) | ((i >> 3) & 1)) : i; }
DEV float silu_bf(float v) { return v * __builtin_amdgcn_rcpf(1.f + __expf(-v)); }

struct __attribute__((aligned(16))) Lds {
  float red[8][64];
  float ored[8][128];
  float x2[D];
  float modv[3][D];
  u32 q[64];
  u32 vec[12][80];
  float mw[8], lw[8], st[8][2];
  u32 cnt[4];
};

DEV void cbar(Lds& L, u32& target, int lane) {
  target += 4;
  lds_wait();
  if (lane == 0) __hip_atomic_fetch_add(&L.cnt[0], 1u, __ATOMIC_RELAXED, __HIP_MEMORY_SCOPE_WORKGROUP);
#pragma unroll 1
  for (u32 s = 0; s < SPIN_CAP; ++s) { fence(); if (__hip_atomic_load(&L.cnt[0], __ATOMIC_RELAXED, __HIP_MEMORY_SCOPE_WORKGROUP) >= target) break; }
  fence();
}
DEV void ln_fin(float s1, float s2, float& mean_b, float& rsd) {
  const float m = s1 * (1.f / D), var = rbf(fmaxf(s2 * (1.f / D) - m * m, 0.f));
  mean_b = rbf(m);
  rsd = __builtin_amdgcn_rcpf(rbf(__builtin_amdgcn_sqrtf(rbf(var + 1e-5f))));
}
DEV float ln_mod(float x, float mean_b, float rsd, float sc, float sh) { return rbf(rbf(rbf(x - mean_b) * rsd) * rbf(1.f + sc)) + sh; }
DEV float lo(u32 v) { return __uint_as_float(v << 16); }
DEV float hi(u32 v) { return __uint_as_float(v & 0xFFFF0000u); }
DEV float gval(u32 v) { return __uint_as_float(v & 0xFFFFFF00u); }
DEV float sum8(const Lds& L, int lane) { return ((L.red[0][lane] + L.red[1][lane]) + (L.red[2][lane] + L.red[3][lane])) + ((L.red[4][lane] + L.red[5][lane]) + (L.red[6][lane] + L.red[7][lane])); }

__global__ __launch_bounds__(768, 3) void adaln_block_kernel(Params P) {
  __shared__ Lds L;
  const int tid = threadIdx.x, lane = tid & 63, b = blockIdx.x;
  const int wave = __builtin_amdgcn_readfirstlane(tid >> 6);
  const int g = lane >> 4, n = lane & 15, t = n >> 3;
  const int h = b >> 3, s = b & 7;
  const bool isSaca = b < N_SACA, isMod = b >= B_MOD && b < B_WO, isWo = b >= B_WO && b < B_MLP, isMlp = b >= B_MLP && b < B_MLP + N_MLP;
  const rsrc_t ws = __builtin_amdgcn_make_buffer_rsrc(P.ws, 0, WS_BYTES, 0x00020000);
  const f32x4 zero4 = {0.f, 0.f, 0.f, 0.f};
  const int fi = b - B_MLP, f0 = 32 * pairmap(fi, N_MLP);
  const u32x2 ep = ld2(ws, WS_EPOCH);
#define SET_TAG const u64 e1 = (((u64)(u32)__builtin_amdgcn_readfirstlane(ep.y) << 32) | (u32)__builtin_amdgcn_readfirstlane(ep.x)) + 1; const u32 tagLo = (u32)e1 & 0xFFu, tagHi = (u32)(e1 >> 8)
  if (wave < 4) {
    const int cw = wave, r8 = 2 * g + t, r20 = 160 * cw + 20 * r8;
    const int wp = (b - B_WO) / 10, wc = (b - B_WO) % 10;
    if (tid == 0) L.cnt[0] = 0;
    if (EARLY0) wg_barrier();
    u32 nb = 0;
    u32x4 W[20], WQ[5];
    u32 cv[10], xv[5];
    const int line = b % 20, kq = b / 20, rs = 160 * kq + 40 * cw + 2 * r8;
    if (isSaca) {
      UNR for (int i = 0; i < 5; ++i) W[i] = ldw<NT_W>(P.wm + (size_t)(i < 4 ? rs + 16 * (i >> 1) + (i & 1) : rs + 32 - r8) * NMOD + 64 * line + 8 * (n & 7));
      cv[0] = ((const u32*)P.c)[rs >> 1]; cv[1] = ((const u32*)P.c)[(rs >> 1) + 8]; cv[2] = ((const u32*)P.c)[(rs + 32 - r8) >> 1];
    }
    const int rq = 80 * s + 20 * cw + 5 * g, mr = rq + n % 5;
    UNR for (int i = 0; i < 5; ++i) xv[i] = ((const u32*)P.x)[lane + 64 * i];
    const u32 xs = ((const u32*)P.x)[mr >> 1];
    UNR for (int i = 0; i < 5; ++i) WQ[i] = ldw<NT_W>(P.wq + (size_t)(rq + i) * 4096 + h * DH + 8 * n);
    if (cw == 3) {
      const u32x4 e = {EMPTY, EMPTY, EMPTY, EMPTY};
      st4(ws, (lane < 32 ? WS_QP : WS_OP - 512) + b * 512 + lane * 16, e);
      if (lane == 0) st4(ws, WS_ML + b * 64, e);
      if (isWo && lane < 16) st4(ws, WS_WOP + (wp * D + wc * 64) * 4 + lane * 16, e);
      if (isMlp) for (int i = lane; i < 160; i += 64) st4(ws, WS_DP + fi * (D * 4) + i * 16, e);
    }
    if (!EARLY0) wg_barrier();
    fence();
    SET_TAG;
    const u32x4 selL = mksel(n & 7, 0), selH = mksel(n & 7, 1);
    if (isSaca) {
      const u32 w0 = pk2(silu_bf(lo(cv[0])), silu_bf(hi(cv[0]))), w1 = pk2(silu_bf(lo(cv[1])), silu_bf(hi(cv[1])));
      const u32 w2 = pk2(silu_bf(t ? hi(cv[2]) : lo(cv[2])), 0.f);
      f32x4 acc = mfma(mkA(w0, selL), W[0], zero4);
      acc = mfma(mkA(w0, selH), W[1], acc);
      acc = mfma(mkA(w1, selL), W[2], acc);
      acc = mfma(mkA(w1, selH), W[3], acc);
      acc = mfma(mkA(w2, selL), W[4], acc);
      if ((g >> 1) == t) *(f32x4*)&L.red[2 * cw + t][8 * (n & 7) + 4 * (g & 1)] = acc;
      cbar(L, nb, lane);
      if (cw == 0) { const u32x2 gr = {((__float_as_uint(sum8(L, lane)) + 0x80u) & 0xFFFFFF00u) | tagLo, tagHi}; st2(ws, WS_H1 + (64 * line + lane) * 32 + kq * 8, gr); }
    }
    if (cw == 3) {
      asm volatile("s_waitcnt vmcnt(0)" ::: "memory");
      if (lane == 0) { const u32x2 f = {tagLo, tagHi}; st2(ws, WS_FLAG + b * 8, f); }
    }
    float mb, rsd;
    {
      float s1 = 0.f, s2 = 0.f;
      UNR for (int i = 0; i < 5; ++i) { const float a = lo(xv[i]), c = hi(xv[i]); s1 += a + c; s2 += a * a + c * c; }
      ln_fin(wave_sum(s1), wave_sum(s2), mb, rsd);
    }
    const bool role = isWo || isMlp || isMod;
    const u16* wsrc = isWo ? P.wo + (size_t)(128 * (4 * wp + cw) + 16 * r8) * D + 64 * wc + 8 * (n & 7)
                    : isMlp ? (((n >> 2) & 1) ? P.wu : P.wg) + (size_t)r20 * DF + f0 + 8 * (n & 3)
                            : P.wm + (size_t)r20 * NMOD + 1280 + 64 * (b - B_MOD) + 8 * (n & 7);
    const int wstr = isWo ? D : isMlp ? DF : NMOD;
    if (role) {
      if (isMod) { UNR for (int i = 0; i < 5; ++i) { const u32x2 v = *(const u32x2*)(P.c + r20 + 4 * i); cv[2 * i] = v.x; cv[2 * i + 1] = v.y; } }
      UNR for (int i = 0; i < PH1; ++i) { W[i] = ldw<NT_W>(wsrc + i * wstr); THROTTLE(WINC); }
    } else if (!isSaca) __builtin_amdgcn_s_sleep(NAP0);
    {
      u32x4 p0, p1;
      const u32 off = WS_H1 + (mr + (n >= 5 && n < 10 ? D : 0)) * 32;
      SPIN(NAP1, SPIN_CAP, const u32x2 f = ld2(ws, cw == 3 ? WS_FLAG + (8 * h + (lane & 7)) * 8 : off); p0 = ld4(ws, off); p1 = ld4(ws, off + 16);
           const bool ok = (TAGBAD(f) | TAGBAD(p0.xy) | TAGBAD(p0.zw) | TAGBAD(p1.xy) | TAGBAD(p1.zw)) == 0)
      const float v = rbf((gval(p0.x) + gval(p0.z)) + (gval(p1.x) + gval(p1.z)));
      const u32 hb = pk2(ln_mod((mr & 1) ? hi(xs) : lo(xs), mb, rsd, dpp<0x105>(v), v), 0.f);
      f32x4 acc = mfma(mkA(dppu<0x150>(hb), selL), WQ[0], zero4);
      acc = mfma(mkA(dppu<0x151>(hb), selL), WQ[1], acc);
      acc = mfma(mkA(dppu<0x152>(hb), selL), WQ[2], acc);
      acc = mfma(mkA(dppu<0x153>(hb), selL), WQ[3], acc);
      acc = mfma(mkA(dppu<0x154>(hb), selL), WQ[4], acc);
      if (g < 2) *(f32x4*)&L.ored[cw][8 * n + 4 * g] = acc;
      cbar(L, nb, lane);
    }
    if (cw == 0) {
      const int l4 = 4 * (lane & 31);
      const f32x4 v0 = (*(f32x4*)&L.ored[0][l4] + *(f32x4*)&L.ored[1][l4]) + (*(f32x4*)&L.ored[2][l4] + *(f32x4*)&L.ored[3][l4]);
      if (lane < 32) st4(ws, WS_QP + b * 512 + lane * 16, canon4(v0));
      u32x4 p[4];
      SPIN(2, SPIN_CAP, UNR for (int k = 0; k < 4; ++k) p[k] = ld4(ws, WS_QP + h * 4096 + k * 1024 + lane * 16); const bool ok = full16(p, 0))
      const f32x4 v = (asf(p[0]) + asf(p[1])) + (asf(p[2]) + asf(p[3]));
      const u32x2 qq = {pk2(sum32(v.x), sum32(v.y)), pk2(sum32(v.z), sum32(v.w))};
      if (lane < 32) *(u32x2*)&L.q[2 * lane] = qq;
    }
    if (cw == 3 && (isWo || isMlp)) {
      SPIN(NAP1, SPIN_CAP, const u32x4 f0v = ld4(ws, WS_FLAG + lane * 32); const u32x4 f1v = ld4(ws, WS_FLAG + lane * 32 + 16);
           const bool ok = (TAGBAD(f0v.xy) | TAGBAD(f0v.zw) | TAGBAD(f1v.xy) | TAGBAD(f1v.zw)) == 0)
    }
    wg_barrier(); wg_barrier();
    if (role) { UNR for (int i = PH1; i < 20; ++i) if ((i < 16 || !isWo) && (i < PH2M || !isMlp)) { W[i] = ldw<NT_W>(wsrc + i * wstr); THROTTLE(WINC2); } }
    if (isMod) { UNR for (int i = 0; i < 10; ++i) { cv[i] = pk2(silu_bf(lo(cv[i])), silu_bf(hi(cv[i]))); asm volatile("" : "+v"(cv[i])); } }
    const float mwg = fmaxf(fmaxf(fmaxf(L.mw[0], L.mw[1]), fmaxf(L.mw[2], L.mw[3])), fmaxf(fmaxf(L.mw[4], L.mw[5]), fmaxf(L.mw[6], L.mw[7])));
    wg_barrier();
    if (cw == 0) {
      const int l4 = 4 * (lane & 31), k0 = 4 * (lane >> 5);
      f32x4 o = (*(f32x4*)&L.ored[k0][l4] + *(f32x4*)&L.ored[k0 + 1][l4]) + (*(f32x4*)&L.ored[k0 + 2][l4] + *(f32x4*)&L.ored[k0 + 3][l4]);
      const f32x4 la = *(f32x4*)&L.lw[0] + *(f32x4*)&L.lw[4];
      o.x = sum32(o.x); o.y = sum32(o.y); o.z = sum32(o.z); o.w = sum32(o.w);
      const u32x2 mlv = {canon(mwg), canon((la.x + la.y) + (la.z + la.w))};
      if (lane < 32) st4(ws, WS_OP + b * 512 + lane * 16, canon4(o));
      if (lane == 32) st2(ws, WS_ML + b * 64, mlv);
    }
    if (isMod) {
      f32x4 acc = zero4;
      UNR for (int i = 0; i < 10; ++i) {
        const u32 w = cv[i];
        acc = mfma(mkA(w, selL), W[2 * i], acc);
        acc = mfma(mkA(w, selH), W[2 * i + 1], acc);
      }
      if ((g >> 1) == t) *(f32x4*)&L.red[2 * cw + t][8 * (n & 7) + 4 * (g & 1)] = acc;
      cbar(L, nb, lane);
      if (cw == 1) { const u32x2 gr = {__float_as_uint(rbf(sum8(L, lane))) | tagLo, tagHi}; st2(ws, WS_MOD + (64 * (b - B_MOD) + lane) * 8, gr); }
    }
    if (isWo) {
      const int hh = 4 * wp + cw;
      u32x4 p[4]; u32x2 mlv;
      SPIN(NAPA, SPIN_CAP, mlv = ld2(ws, WS_ML + (hh * 8 + (lane & 7)) * 64);
           UNR for (int k = 0; k < 4; ++k) p[k] = ld4(ws, WS_OP + hh * 4096 + k * 1024 + lane * 16);
           const bool ok = full16(p, max(mlv.x, mlv.y)))
      const float mw = __uint_as_float(mlv.x), lw = __uint_as_float(mlv.y);
      float m = fmaxf(mw, dpp<0xB1>(mw)); m = fmaxf(m, dpp<0x4E>(m)); m = fmaxf(m, dpp<0x141>(m));
      float e = __expf(mw - m);
      float l = quad_sum(e * lw); l += dpp<0x141>(l);
      e *= __builtin_amdgcn_rcpf(l);
      e = lane < 32 ? e : dpp<0xB1>(e);
      const f32x4 o = (dpp<0x150>(e) * asf(p[0]) + dpp<0x152>(e) * asf(p[1])) + (dpp<0x154>(e) * asf(p[2]) + dpp<0x156>(e) * asf(p[3]));
      const u32x2 ob = {pk2(sum32(o.x), sum32(o.y)), pk2(sum32(o.z), sum32(o.w))};
      if (lane < 32) *(u32x2*)&L.vec[wave][2 * lane] = ob;
      lds_wait();
      const u32x4 m0 = *(u32x4*)&L.vec[wave][8 * r8], m1 = *(u32x4*)&L.vec[wave][8 * r8 + 4];
      f32x4 acc = zero4;
      UNR for (int i = 0; i < 8; ++i) {
        const u32 w = i < 4 ? m0[i & 3] : m1[i & 3];
        acc = mfma(mkA(w, selL), W[2 * i], acc);
        acc = mfma(mkA(w, selH), W[2 * i + 1], acc);
      }
      if ((g >> 1) == t) *(f32x4*)&L.red[2 * cw + t][8 * (n & 7) + 4 * (g & 1)] = acc;
      cbar(L, nb, lane);
      if (cw == 0) st1(ws, WS_WOP + (wp * D + 64 * wc + lane) * 4, canon(sum8(L, lane)));
    }
    if (isMlp) {
      if (cw != 0) __builtin_amdgcn_s_sleep(8);
      UNR for (int i = PH2M; i < 20; ++i) W[i] = ldw<NT_W>(wsrc + i * wstr);
      wg_barrier();
      const int r = 160 * cw + 4 * (lane < 40 ? lane : 0);
      const f32x4 s01 = *(f32x4*)&L.st[0][0], s23 = *(f32x4*)&L.st[2][0], x2 = *(f32x4*)&L.x2[r], sb = *(f32x4*)&L.modv[0][r], cb = *(f32x4*)&L.modv[1][r];
      ln_fin((s01.x + s01.z) + (s23.x + s23.z) + L.st[4][0], (s01.y + s01.w) + (s23.y + s23.w) + L.st[4][1], mb, rsd);
      const u32x2 hb = {pk2(ln_mod(x2.x, mb, rsd, cb.x, sb.x), ln_mod(x2.y, mb, rsd, cb.y, sb.y)), pk2(ln_mod(x2.z, mb, rsd, cb.z, sb.z), ln_mod(x2.w, mb, rsd, cb.w, sb.w))};
      if (lane < 40) *(u32x2*)&L.vec[wave][2 * lane] = hb;
      lds_wait();
      f32x4 acc = zero4;
      UNR for (int i = 0; i < 5; ++i) {
        const u32x2 w = *(u32x2*)&L.vec[wave][10 * r8 + 2 * i];
        acc = mfma(mkA(w.x, selL), W[4 * i], acc);
        acc = mfma(mkA(w.x, selH), W[4 * i + 1], acc);
        acc = mfma(mkA(w.y, selL), W[4 * i + 2], acc);
        acc = mfma(mkA(w.y, selH), W[4 * i + 3], acc);
      }
      if ((g >> 1) == t) *(f32x4*)&L.red[2 * cw + t][8 * (n & 7) + 4 * (g & 1)] = acc;
      wg_barrier();
      if (cw == 0 && fi < N_FIN) {
        u32x4 p[4];
        const int pp = lane >> 2, col = 16 * fi + 4 * (lane & 3);
        const bool has3 = pp + 48 < N_MLP;
        const f32x4 xo = *(f32x4*)&L.x2[col], gb = *(f32x4*)&L.modv[2][col];
        SPIN(NAPC, SPIN_CAP, UNR for (int k = 0; k < 4; ++k) p[k] = ld4(ws, WS_DP + col * 4 + (k < 3 || has3 ? pp + 16 * k : pp) * (D * 4)); const bool ok = full16(p, 0))
        const f32x4 y = (asf(p[0]) + asf(p[1])) + (asf(p[2]) + (has3 ? asf(p[3]) : zero4));
        const u32x2 o = {pk2(xo.x + rbf(gb.x * rbf(top_sum(y.x))), xo.y + rbf(gb.y * rbf(top_sum(y.y)))),
                         pk2(xo.z + rbf(gb.z * rbf(top_sum(y.z))), xo.w + rbf(gb.w * rbf(top_sum(y.w))))};
        if (lane < 4) *(u32x2*)(P.out + col) = o;
        if (fi == 0 && lane == 0) { const u32x2 e = {(u32)e1, (u32)(e1 >> 32)}; st2(ws, WS_EPOCH, e); }
      }
    }
  } else {
    const int sw = wave - 4;
    const size_t kb = ((size_t)h * SK + 512 * s + 64 * sw) * DH;
    const u16* kp = P.kc + kb + (n >> 2) * DH + 32 * (n & 3) + 8 * g;
    const u16* vp = P.vc + kb + 4 * g * DH + 8 * n;
    u32x4 K[16], V[16];
    wg_barrier();
    #define THR_S do { if (isMlp) THROTTLE(WINH); else if (isSaca || b >= B_MLP) THROTTLE(WIN); else THROTTLE(WINM); } while (0)
    UNR for (int i = 0; i < 16; ++i) { K[i] = ldw<NT_KV>(kp + i * 4 * DH); THR_S; }
    SET_TAG;
    UNR for (int i = 0; i < VQ; ++i) { V[i] = ldw<NT_KV>(vp + ((i >> 2) * 16 + (i & 3)) * DH); THR_S; }
    wg_barrier();
    UNR for (int i = VQ; i < VQ + VQ2; ++i) V[i] = ldw<NT_KV>(vp + ((i >> 2) * 16 + (i & 3)) * DH);
    fence();
    float sc[4];
    {
      const u32x4 aq = *(u32x4*)&L.q[16 * (n & 3) + 4 * g], z = {0u, 0u, 0u, 0u};
      u32x4 A[4];
      UNR for (int k = 0; k < 4; ++k) A[k] = (n >> 2) == k ? aq : z;
      UNR for (int G = 0; G < 4; ++G) {
        f32x4 acc = zero4;
        UNR for (int k = 0; k < 4; ++k) acc = mfma(A[k], K[4 * G + k], acc);
        const float v = (n & 2) ? ((n & 1) ? acc.w : acc.z) : ((n & 1) ? acc.y : acc.x);
        sc[G] = rbf(rbf(quad_sum(v)) * 0.08838834764831845f);
      }
    }
    const float mv = top_max(fmaxf(fmaxf(sc[0], sc[1]), fmaxf(sc[2], sc[3])));
    if (lane == 0) L.mw[sw] = mv;
    wg_barrier();
    const f32x4 ma = *(f32x4*)&L.mw[0], mc = *(f32x4*)&L.mw[4];
    const float m = fmaxf(fmaxf(fmaxf(ma.x, ma.y), fmaxf(ma.z, ma.w)), fmaxf(fmaxf(mc.x, mc.y), fmaxf(mc.z, mc.w)));
    u32 pw[4];
    float l = 0.f;
    UNR for (int G = 0; G < 4; ++G) { const float p = __expf(sc[G] - m), ph = rbf(p); l += p; pw[G] = pk2(ph, p - ph); }
    l = top_sum(l);
    if (lane == 0) L.lw[sw] = l;
    fence();
    UNR for (int i = VQ + VQ2; i < 16; ++i) { V[i] = ldw<NT_KV>(vp + ((i >> 2) * 16 + (i & 3)) * DH); if (i < 15) THR_S; }
    fence();
    const u32x4 selP = mksel(n & 7, t);
    f32x4 acc = zero4;
    UNR for (int G = 0; G < 4; ++G) {
      acc = mfma(mkA(dppu<0x150>(pw[G]), selP), V[4 * G], acc);
      acc = mfma(mkA(dppu<0x154>(pw[G]), selP), V[4 * G + 1], acc);
      acc = mfma(mkA(dppu<0x158>(pw[G]), selP), V[4 * G + 2], acc);
      acc = mfma(mkA(dppu<0x15C>(pw[G]), selP), V[4 * G + 3], acc);
    }
    acc.x = sum32(acc.x); acc.y = sum32(acc.y); acc.z = sum32(acc.z); acc.w = sum32(acc.w);
    if (lane < 32) *(f32x4*)&L.ored[sw][8 * n + 4 * g] = acc;
    wg_barrier();
    if (isMlp) {
      if (sw < 5) {
        __builtin_amdgcn_s_sleep(8);
        const u16* src = P.wd + (size_t)(f0 + 8 * g) * D + 128 * sw + 8 * n;
        UNR for (int i = 0; i < 8 - DLATE; ++i) K[i] = ldw<NT_W>(src + i * D);
        u32x4 p[4], g0, g1;
        const int col = 128 * sw + 4 * (lane & 31);
        const u32x2 xx = *(const u32x2*)(P.x + col);
        SPIN(NAPB, SPIN_CAP, g0 = ld4(ws, WS_MOD + col * 8); g1 = ld4(ws, WS_MOD + col * 8 + 16);
             UNR for (int k = 0; k < 4; ++k) p[k] = ld4(ws, WS_WOP + ((2 * k + (lane >> 5)) * D + col) * 4);
             const bool ok = (TAGBAD(g0.xy) | TAGBAD(g0.zw) | TAGBAD(g1.xy) | TAGBAD(g1.zw)) == 0 && full16(p, 0))
        UNR for (int i = 8 - DLATE; i < 8; ++i) K[i] = ldw<NT_W>(src + i * D);
        fence();
        const f32x4 a = (asf(p[0]) + asf(p[1])) + (asf(p[2]) + asf(p[3]));
        f32x4 x2;
        x2.x = rbf(lo(xx.x) + rbf(gval(g0.x) * rbf(sum32(a.x))));
        x2.y = rbf(hi(xx.x) + rbf(gval(g0.z) * rbf(sum32(a.y))));
        x2.z = rbf(lo(xx.y) + rbf(gval(g1.x) * rbf(sum32(a.z))));
        x2.w = rbf(hi(xx.y) + rbf(gval(g1.z) * rbf(sum32(a.w))));
        if (lane < 32) *(f32x4*)&L.x2[col] = x2;
        float sv = lane < 32 ? (x2.x + x2.y) + (x2.z + x2.w) : (x2.x * x2.x + x2.y * x2.y) + (x2.z * x2.z + x2.w * x2.w);
        sv = quad_sum(sv); sv += dpp<0x124>(sv); sv += dpp<0x128>(sv); sv = sum16(sv);
        if ((lane & 31) == 0) L.st[sw][lane >> 5] = sv;
      } else {
        u32x4 mg[5];
        SPIN(2, SPIN_CAP, u32 bad = 0;
             UNR for (int i = 0; i < 5; ++i) mg[i] = ld4(ws, WS_MOD + ((sw - 4) * D + 2 * lane + 128 * i) * 8);
             UNR for (int i = 0; i < 5; ++i) bad |= TAGBAD(mg[i].xy) | TAGBAD(mg[i].zw);
             const bool ok = bad == 0)
        UNR for (int i = 0; i < 5; ++i) { const f32x2 v = {gval(mg[i].x), gval(mg[i].z)}; *(f32x2*)&L.modv[sw - 5][2 * lane + 128 * i] = v; }
      }
      const u32x4 selL = mksel(n & 7, 0), selH = mksel(n & 7, 1);
      wg_barrier();
      wg_barrier();
      if (sw < 5) {
        const float v = rbf(sum8(L, lane));
        auto r = __builtin_amdgcn_permlane32_swap(__float_as_uint(v), __float_as_uint(v), false, false);
        const float gv = __uint_as_float(r[0]), uv = __uint_as_float(r[1]);
        const float in2 = (2.f * 0.7978845608028654f) * (gv + 0.044715f * gv * gv * gv);
        const float th = 1.f - 2.f * __builtin_amdgcn_rcpf(__expf(in2) + 1.f);
        const u32 hb = pk2(rbf(0.5f * gv * (1.f + th)) * uv, 0.f);
        if (lane < 32) ((u16*)L.vec[wave])[lane] = (u16)hb;
        lds_wait();
        const u32x4 mm = *(u32x4*)&L.vec[wave][4 * g];
        f32x4 y = zero4;
        UNR for (int i = 0; i < 4; ++i) { y = mfma(mkA(mm[i], selL), K[2 * i], y); y = mfma(mkA(mm[i], selH), K[2 * i + 1], y); }
        if (g < 2) st4(ws, WS_DP + (fi * D + 128 * sw + 8 * n + 4 * g) * 4, canon4(y));
      }
    }
  }
}

extern "C" void kernel_launch(void* const* d_in, const int* in_sizes, int n_in, void* d_out, int out_size,
                              void* d_ws, size_t ws_size, hipStream_t stream) {
  Params p;
  p.x = (const u16*)d_in[0]; p.kc = (const u16*)d_in[1]; p.vc = (const u16*)d_in[2];
  p.wq = (const u16*)d_in[3]; p.wo = (const u16*)d_in[4];
  p.wg = (const u16*)d_in[6]; p.wu = (const u16*)d_in[7]; p.wd = (const u16*)d_in[8];
  p.c = (const u16*)d_in[9]; p.wm = (const u16*)d_in[10];
  p.out = (u16*)d_out; p.ws = (char*)d_ws;
  adaln_block_kernel<<<256, 768, 0, stream>>>(p);
}
```
